```python
import torch

def _softmax(x, axis=-1):
    return torch.softmax(x, dim=axis)
def _gelu(x):
    return torch.nn.functional.gelu(x, approximate="tanh")
def _silu(x):
    return torch.nn.functional.silu(x)
def _sigmoid(x):
    return torch.sigmoid(x)
def _layernorm(x, axis=-1, eps=1e-5):
    m = x.mean(dim=axis, keepdim=True)
    v = x.var(dim=axis, keepdim=True, unbiased=False)
    return (x - m) / torch.sqrt(v + eps)
def _scatter_add(tgt, idx, src, axis=0):
    return tgt.clone().index_add_(axis, idx, src)
def _segment_sum(src, seg, n):
    out = torch.zeros((n,) + src.shape[1:], dtype=src.dtype)
    return out.index_add_(0, seg, src)
def _pad(x, pw):
    flat = tuple(v for pair in reversed(pw) for v in pair)
    return torch.nn.functional.pad(x, flat)

kernel_name = 'parallel_sum__grouped_kv_mqa__glu_gated_bilinear__prompt'

def setup_inputs():

    _w = (lambda a: (lambda t: t.to(torch.bfloat16) if t.is_floating_point() else t)(torch.as_tensor(a)))
    def _A_setup():
        B, S, D, H = 1, 2048, 448, 16
        Dh = D // H
        return {
            "x": _w((lambda *s: torch.randn(*s, dtype=torch.bfloat16) * 0.05)(B, S, D)),
            "wq": _w((lambda *s: torch.randn(*s, dtype=torch.bfloat16) * 0.05)(D, H * Dh)),
            "wk": _w((lambda *s: torch.randn(*s, dtype=torch.bfloat16) * 0.05)(D, Dh)),
            "wv": _w((lambda *s: torch.randn(*s, dtype=torch.bfloat16) * 0.05)(D, Dh)),
            "wo": _w((lambda *s: torch.randn(*s, dtype=torch.bfloat16) * 0.05)(H * Dh, D)),
        }
    _A_in = _A_setup()

    def _B_setup():
        B, S, D, F = 1, 2048, 448, 1792
        return {
            "x": _w((lambda *s: torch.randn(*s, dtype=torch.bfloat16) * 0.05)(B, S, D)),
            "w_gate": _w((lambda *s: torch.randn(*s, dtype=torch.bfloat16) * 0.4)(D, F) * (D ** -0.5)),
            "w_up": _w((lambda *s: torch.randn(*s, dtype=torch.bfloat16) * 0.4)(D, F) * (D ** -0.5)),
            "w_down": _w((lambda *s: torch.randn(*s, dtype=torch.bfloat16) * 0.4)(F, D) * (F ** -0.5)),
        }
    _B_in = _B_setup()

    return {
        "A_x": _A_in["x"],
        "A_wq": _A_in["wq"],
        "A_wk": _A_in["wk"],
        "A_wv": _A_in["wv"],
        "A_wo": _A_in["wo"],
        "B_x": _B_in["x"],
        "B_w_gate": _B_in["w_gate"],
        "B_w_up": _B_in["w_up"],
        "B_w_down": _B_in["w_down"],
    }

def reference(A_x, A_wq, A_wk, A_wv, A_wo, B_x, B_w_gate, B_w_up, B_w_down):
    def _A_ref(x, wq, wk, wv, wo):
        B, S, D = x.shape
        H = 16
        Dh = D // H
        q = (lambda x, *d: x.permute(*d))((x @ wq).reshape(B, S, H, Dh), 0, 2, 1, 3)
        k = x @ wk
        v = x @ wv
        scores = torch.einsum("bhqd,bkd->bhqk", q, k) * (Dh ** -0.5)
        p = _softmax(scores, axis=-1)
        o = torch.einsum("bhqk,bkd->bhqd", p, v)
        return (lambda x, *d: x.permute(*d))(o, 0, 2, 1, 3).reshape(B, S, H * Dh) @ wo

    def _B_ref(x, w_gate, w_up, w_down):
        g = torch.einsum("bsd,df->bsf", x, w_gate)
        u = torch.einsum("bsd,df->bsf", x, w_up)
        h = g * u
        return torch.einsum("bsf,fd->bsd", h, w_down)

    x = A_x
    xn = _layernorm(x, axis=-1)
    return x + _A_ref(xn, A_wq, A_wk, A_wv, A_wo) + _B_ref(xn, B_w_gate, B_w_up, B_w_down)
```

```cpp
#include <hip/hip_runtime.h>
#include <hip/hip_bf16.h>
#include <stdint.h>

typedef short bf16x8 __attribute__((ext_vector_type(8)));
typedef short s16x4 __attribute__((ext_vector_type(4)));
typedef __bf16 bf16x2_t __attribute__((ext_vector_type(2)));
typedef float f32x16 __attribute__((ext_vector_type(16)));
typedef float f32x8 __attribute__((ext_vector_type(8)));
typedef float f32x4 __attribute__((ext_vector_type(4)));
typedef float f32x2 __attribute__((ext_vector_type(2)));
typedef unsigned u32x4 __attribute__((ext_vector_type(4)));
typedef unsigned u32x2 __attribute__((ext_vector_type(2)));
typedef unsigned u32x8 __attribute__((ext_vector_type(8)));
#define LAS __attribute__((address_space(3)))
#define GAS __attribute__((address_space(1)))
#define DI __device__ __forceinline__
#define MFMA(a, b, c) __builtin_amdgcn_mfma_f32_32x32x16_bf16((a), (b), (c), 0, 0, 0)
#define SBAR() __builtin_amdgcn_sched_barrier(0)

constexpr int S_ = 2048, D_ = 448, F_ = 1792, DH = 28;
constexpr int NKS = 144;
constexpr size_t WS_HO = 0;
constexpr size_t WS_Q = WS_HO + (size_t)64 * NKS * 1024;
constexpr size_t WS_KV = WS_Q + (size_t)2048 * 1024;
constexpr float QSCALE = 0.18898223650461362f * 1.4426950408889634f;

DI unsigned cvtpk(float lo, float hi) { f32x2 v = {lo, hi}; return __builtin_bit_cast(unsigned, __builtin_convertvector(v, bf16x2_t)); }
DI float dot2(unsigned a, unsigned b, float c) { return __builtin_amdgcn_fdot2_f32_bf16(__builtin_bit_cast(bf16x2_t, a), __builtin_bit_cast(bf16x2_t, b), c, false); }
DI void glds16(const void* g, unsigned lds_base) {
  asm volatile("s_mov_b32 m0, %1\n\ts_nop 0\n\tglobal_load_lds_dwordx4 %0, off" :: "v"(g), "s"(lds_base) : "memory"); }
DI s16x4 trrd(LAS const char* p) { return __builtin_amdgcn_ds_read_tr16_b64_v4i16((LAS s16x4*)p); }
DI bf16x8 cat(s16x4 a, s16x4 b) { return __builtin_shufflevector(a, b, 0, 1, 2, 3, 4, 5, 6, 7); }
DI float swapadd(float v) { auto r = __builtin_amdgcn_permlane32_swap(__float_as_uint(v), __float_as_uint(v), false, false); return __uint_as_float(r[0]) + __uint_as_float(r[1]); }
DI float swapmax(float v) { auto r = __builtin_amdgcn_permlane32_swap(__float_as_uint(v), __float_as_uint(v), false, false); return fmaxf(__uint_as_float(r[0]), __uint_as_float(r[1])); }

constexpr int K1_STAGE = 48 * 1024, K1_LDS = 3 * K1_STAGE + 4096;
__global__ __launch_bounds__(256, 1) void k1(const char* __restrict__ x, const char* __restrict__ wq, const char* __restrict__ wk, const char* __restrict__ wv,
                                             const char* __restrict__ wg, const char* __restrict__ wu, char* __restrict__ ws) {
  extern __shared__ __attribute__((aligned(16))) char smem[];
  LAS char* lds = (LAS char*)smem;
  const unsigned lds0 = (unsigned)(uintptr_t)lds;
  const int tid = threadIdx.x, lane = tid & 63, w = __builtin_amdgcn_readfirstlane(tid >> 6), r32 = lane & 31, hi = lane >> 5;
  const int xcd = blockIdx.x & 7, ii = blockIdx.x >> 3, tb = (xcd & 1) * 8 + (ii & 7), j = (xcd >> 1) * 4 + (ii >> 3);
  const char* xs0; const char* xs1;
  { const int row = 32 * w + (lane >> 3); const char* b = x + (size_t)(tb * 128 + row) * 896;
    xs0 = b + (((lane & 7) ^ (lane >> 4)) << 4); xs1 = b + 8 * 896 + (((lane & 7) ^ (4 + (lane >> 4))) << 4); }
  const char* a0; const char* a1; unsigned p1;
  { const int row = lane >> 2, c = lane & 3;
    if (w < 3) { const size_t o = (size_t)row * 3584 + (3 * j + w) * 64 + c * 16; a0 = wg + o; a1 = wu + o; p1 = 3584; }
    else { a0 = (c < 2 ? wg : wu) + (size_t)row * 3584 + 3072 + j * 32 + (c & 1) * 16; a1 = wq + (size_t)row * 896 + ((56 * j) & ~15) + c * 16; p1 = 896; } }
  u32x2 kvq[7];
  { const char* kvs = (j < 7 ? wk + 8 * j : wv + 8 * (j < 14 ? j - 7 : 0)) + lane * 56;
#pragma unroll
    for (int s = 0; s < 7; ++s) asm volatile("global_load_dwordx2 %0, %1, off" : "=v"(kvq[s]) : "v"(kvs + s * 64 * 56) : "memory"); }
  auto dma = [&](int s, int slot, int i) {
    const unsigned base = lds0 + slot * K1_STAGE;
    if (i < 4) glds16(((i & 1) ? xs1 : xs0) + (i >> 1) * 16 * 896 + s * 128, base + w * 4096 + i * 1024);
    else if (i < 8) glds16(a0 + (size_t)(s * 64 + (i - 4) * 16) * 3584, base + 16384 + w * 8192 + (i - 4) * 2048);
    else glds16(a1 + (size_t)(s * 64 + (i - 8) * 16) * p1, base + 16384 + w * 8192 + (i - 8) * 2048 + 1024);
  };
#pragma unroll
  for (int s = 0; s < 3; ++s)
#pragma unroll
    for (int i = 0; i < 12; ++i) dma(s, s, i);
  const int trrow = (8 * hi + ((lane & 15) >> 2)) * 64, cq = ((lane >> 4) & 1) * 4 + (lane & 3);
  const int aoff0 = 16384 + w * 8192 + trrow + cq * 8, aoff1 = 16384 + w * 8192 + 1024 + trrow + (w == 3 ? ((cq + (j & 1)) & 7) : cq) * 8;
  int boff[4];
#pragma unroll
  for (int kk = 0; kk < 4; ++kk) boff[kk] = r32 * 128 + (((2 * kk + hi) ^ ((r32 >> 1) & 7)) << 4);
  const int kvoff = 16384 + 3 * 8192 + 1024 + (lane >> 4) * 2048 + (lane & 15) * 64 + ((j & 1) ? 0 : 56);
  f32x16 acc[2][4];
#pragma unroll
  for (int t = 0; t < 2; ++t)
#pragma unroll
    for (int b = 0; b < 4; ++b) acc[t][b] = f32x16{};
  float sx = 0.f, sxx = 0.f, cs0 = 0.f, cs1 = 0.f;
  bf16x8 fa[2][2], fb[2][4];
  auto rd = [&](int buf, int slot, int kk) {
    LAS const char* p = lds + slot * K1_STAGE;
    fa[buf][0] = cat(trrd(p + aoff0 + kk * 2048), trrd(p + aoff0 + kk * 2048 + 256));
    fa[buf][1] = cat(trrd(p + aoff1 + kk * 2048), trrd(p + aoff1 + kk * 2048 + 256));
#pragma unroll
    for (int b = 0; b < 4; ++b) fb[buf][b] = *(LAS const bf16x8*)(p + boff[kk] + ((w + b) & 3) * 4096);
  };
  auto mm = [&](int buf) {
#pragma unroll
    for (int b = 0; b < 4; ++b) { acc[0][b] = MFMA(fa[buf][0], fb[buf][b], acc[0][b]); acc[1][b] = MFMA(fa[buf][1], fb[buf][b], acc[1][b]); }
    const u32x4 xb = __builtin_bit_cast(u32x4, fb[buf][0]), w0 = __builtin_bit_cast(u32x4, fa[buf][0]), w1 = __builtin_bit_cast(u32x4, fa[buf][1]);
#pragma unroll
    for (int i = 0; i < 4; ++i) { sx = dot2(xb[i], 0x3f803f80u, sx); sxx = dot2(xb[i], xb[i], sxx); cs0 = dot2(w0[i], 0x3f803f80u, cs0); cs1 = dot2(w1[i], 0x3f803f80u, cs1); }
    asm volatile("" : "+v"(sx), "+v"(sxx), "+v"(cs0), "+v"(cs1));
  };
  asm volatile("s_waitcnt vmcnt(24)" : "+v"(kvq[0]), "+v"(kvq[1]), "+v"(kvq[2]), "+v"(kvq[3]), "+v"(kvq[4]), "+v"(kvq[5]), "+v"(kvq[6]) :: "memory");
  if (w == 3) *(LAS u32x2*)(lds + kvoff) = kvq[0];
  asm volatile("s_waitcnt lgkmcnt(0)\n\ts_barrier" ::: "memory");
  rd(0, 0, 0);
#pragma unroll
  for (int s = 0; s < 7; ++s) {
    const int slot = s % 3;
    rd(1, slot, 1); mm(0); SBAR();
    rd(0, slot, 2); mm(1); SBAR();
    rd(1, slot, 3); mm(0); SBAR();
    if (s < 6) {
      if (s < 5) asm volatile("s_waitcnt vmcnt(12)" ::: "memory"); else asm volatile("s_waitcnt vmcnt(0)" ::: "memory");
      if (w == 3) *(LAS u32x2*)(lds + ((s + 1) % 3) * K1_STAGE + kvoff) = kvq[s + 1];
      asm volatile("s_waitcnt lgkmcnt(0)\n\ts_barrier" ::: "memory");
      rd(0, (s + 1) % 3, 0);
      if (s + 3 < 7) {
#pragma unroll
        for (int i = 0; i < 12; ++i) dma(s + 3, slot, i);
      }
    }
    mm(1); SBAR();
  }
  LAS float* st = (LAS float*)(lds + 3 * K1_STAGE);
  sx = swapadd(sx); sxx = swapadd(sxx); cs0 = swapadd(cs0); cs1 = swapadd(cs1);
  { const float m = sx * (1.f / 448.f), var = fmaxf(sxx * (1.f / 448.f) - m * m, 0.f);
    st[hi * 128 + 32 * w + r32] = hi ? __builtin_amdgcn_rsqf(var + 1e-5f) : m;
    st[256 + 64 * w + lane] = hi ? cs1 : cs0; }
  __syncthreads();
  f32x16 c0, c1;
#pragma unroll
  for (int g = 0; g < 4; ++g) { const f32x4 u = *(LAS f32x4*)(st + 256 + 64 * w + 8 * g + 4 * hi), v = *(LAS f32x4*)(st + 256 + 64 * w + 32 + 8 * g + 4 * hi);
#pragma unroll
    for (int i = 0; i < 4; ++i) { c0[4 * g + i] = u[i]; c1[4 * g + i] = v[i]; } }
  LAS short* vt = (LAS short*)lds;
#pragma unroll
  for (int b = 0; b < 4; ++b) {
    const int gb = (w + b) & 3; const float m = st[32 * gb + r32], rs = st[128 + 32 * gb + r32];
    f32x16 g = acc[0][b] - m * c0, u = acc[1][b] - m * c1;
    char* ho = ws + WS_HO + (size_t)(tb * 4 + gb) * NKS * 1024 + lane * 16;
    if (w < 3) {
      const f32x16 h = g * u * (rs * rs);
#pragma unroll
      for (int s = 0; s < 2; ++s) *(u32x4*)(ho + ((3 * j + w) * 2 + s) * 1024) = (u32x4){cvtpk(h[8 * s], h[8 * s + 1]), cvtpk(h[8 * s + 2], h[8 * s + 3]), cvtpk(h[8 * s + 4], h[8 * s + 5]), cvtpk(h[8 * s + 6], h[8 * s + 7])};
    } else {
      float h[8];
#pragma unroll
      for (int i = 0; i < 8; ++i) h[i] = g[i] * g[i + 8] * (rs * rs);
      *(u32x4*)(ho + (96 + j) * 1024) = (u32x4){cvtpk(h[0], h[1]), cvtpk(h[2], h[3]), cvtpk(h[4], h[5]), cvtpk(h[6], h[7])};
      const f32x16 q = u * (rs * QSCALE);
      char* qo = ws + WS_Q + (size_t)(((tb * 16 + j) * 4 + gb) * 2) * 1024 + lane * 16;
      *(u32x4*)qo = (u32x4){cvtpk(q[0], q[1]), cvtpk(q[2], q[3]), cvtpk(q[4], q[5]), cvtpk(q[6], q[7])};
      *(u32x4*)(qo + 1024) = (u32x4){cvtpk(q[8], q[9]), cvtpk(q[10], q[11]), hi ? 0u : cvtpk(q[12], q[13]), hi ? 0u : cvtpk(q[14], q[15])};
      u32x2 kv = {cvtpk(u[12] * rs, u[13] * rs), cvtpk(u[14] * rs, u[15] * rs)};
      char* kvo = ws + WS_KV + (size_t)(tb * 4 + gb) * 4096;
      if (j < 7 || j == 14) { const int dq = j < 7 ? j : 7; if (j == 14) kv = (u32x2){0u, 0u};
        if (hi) *(u32x2*)(kvo + (dq >> 2) * 1024 + ((dq & 1) * 32 + r32) * 16 + ((dq >> 1) & 1) * 8) = kv; }
      else { if (j == 15) kv = (u32x2){0x3f80u, 0u};
        if (hi) { vt[0 * 128 + 32 * gb + r32] = kv[0]; vt[1 * 128 + 32 * gb + r32] = kv[0] >> 16; vt[2 * 128 + 32 * gb + r32] = kv[1]; vt[3 * 128 + 32 * gb + r32] = kv[1] >> 16; } }
    }
  }
  asm volatile("s_waitcnt lgkmcnt(0)" ::: "memory");
  if (w == 3 && j >= 7 && j != 14) {
    const int dq = j == 15 ? 7 : j - 7, d = 4 * dq + (lane >> 4), tg = lane & 15;
    const u32x4 v = *(LAS u32x4*)(vt + (lane >> 4) * 128 + tg * 8);
    *(u32x4*)(ws + WS_KV + (size_t)(tb * 4 + (tg >> 2)) * 4096 + 2048 + ((tg >> 1) & 1) * 1024 + ((tg & 1) * 32 + d) * 16) = v;
  }
}

#define ATTN_ASM \
  "s_mov_b64 s[20:21], %[kv]\n\t" \
  "s_mov_b32 s22, 4\n\t" \
  "global_load_dwordx4 v[192:195], v31, s[20:21] offset:0\n\t" \
  "global_load_dwordx4 v[196:199], v31, s[20:21] offset:1024\n\t" \
  "global_load_dwordx4 v[200:203], v31, s[20:21] offset:2048\n\t" \
  "global_load_dwordx4 v[204:207], v31, s[20:21] offset:3072\n\t" \
  "s_add_u32 s20, s20, 4096\n\t" \
  "s_addc_u32 s21, s21, 0\n\t" \
  "global_load_dwordx4 v[208:211], v31, s[20:21] offset:0\n\t" \
  "global_load_dwordx4 v[212:215], v31, s[20:21] offset:1024\n\t" \
  "global_load_dwordx4 v[216:219], v31, s[20:21] offset:2048\n\t" \
  "global_load_dwordx4 v[220:223], v31, s[20:21] offset:3072\n\t" \
  "s_add_u32 s20, s20, 4096\n\t" \
  "s_addc_u32 s21, s21, 0\n\t" \
  "global_load_dwordx4 v[224:227], v31, s[20:21] offset:0\n\t" \
  "global_load_dwordx4 v[228:231], v31, s[20:21] offset:1024\n\t" \
  "global_load_dwordx4 v[232:235], v31, s[20:21] offset:2048\n\t" \
  "global_load_dwordx4 v[236:239], v31, s[20:21] offset:3072\n\t" \
  "s_add_u32 s20, s20, 4096\n\t" \
  "s_addc_u32 s21, s21, 0\n\t" \
  "v_mov_b32 v30, 0xdddddddd\n\t" \
  "v_mov_b32 v64, 0\n\t" \
  "v_mov_b32 v65, 0\n\t" \
  "v_mov_b32 v66, 0\n\t" \
  "v_mov_b32 v67, 0\n\t" \
  "v_mov_b32 v68, 0\n\t" \
  "v_mov_b32 v69, 0\n\t" \
  "v_mov_b32 v70, 0\n\t" \
  "v_mov_b32 v71, 0\n\t" \
  "v_mov_b32 v72, 0\n\t" \
  "v_mov_b32 v73, 0\n\t" \
  "v_mov_b32 v74, 0\n\t" \
  "v_mov_b32 v75, 0\n\t" \
  "v_mov_b32 v76, 0\n\t" \
  "v_mov_b32 v77, 0\n\t" \
  "v_mov_b32 v78, 0\n\t" \
  "v_mov_b32 v79, 0\n\t" \
  "v_mov_b32 v80, 0\n\t" \
  "v_mov_b32 v81, 0\n\t" \
  "v_mov_b32 v82, 0\n\t" \
  "v_mov_b32 v83, 0\n\t" \
  "v_mov_b32 v84, 0\n\t" \
  "v_mov_b32 v85, 0\n\t" \
  "v_mov_b32 v86, 0\n\t" \
  "v_mov_b32 v87, 0\n\t" \
  "v_mov_b32 v88, 0\n\t" \
  "v_mov_b32 v89, 0\n\t" \
  "v_mov_b32 v90, 0\n\t" \
  "v_mov_b32 v91, 0\n\t" \
  "v_mov_b32 v92, 0\n\t" \
  "v_mov_b32 v93, 0\n\t" \
  "v_mov_b32 v94, 0\n\t" \
  "v_mov_b32 v95, 0\n\t" \
  "v_mov_b32 v96, 0\n\t" \
  "v_mov_b32 v97, 0\n\t" \
  "v_mov_b32 v98, 0\n\t" \
  "v_mov_b32 v99, 0\n\t" \
  "v_mov_b32 v100, 0\n\t" \
  "v_mov_b32 v101, 0\n\t" \
  "v_mov_b32 v102, 0\n\t" \
  "v_mov_b32 v103, 0\n\t" \
  "v_mov_b32 v104, 0\n\t" \
  "v_mov_b32 v105, 0\n\t" \
  "v_mov_b32 v106, 0\n\t" \
  "v_mov_b32 v107, 0\n\t" \
  "v_mov_b32 v108, 0\n\t" \
  "v_mov_b32 v109, 0\n\t" \
  "v_mov_b32 v110, 0\n\t" \
  "v_mov_b32 v111, 0\n\t" \
  "v_mov_b32 v112, 0\n\t" \
  "v_mov_b32 v113, 0\n\t" \
  "v_mov_b32 v114, 0\n\t" \
  "v_mov_b32 v115, 0\n\t" \
  "v_mov_b32 v116, 0\n\t" \
  "v_mov_b32 v117, 0\n\t" \
  "v_mov_b32 v118, 0\n\t" \
  "v_mov_b32 v119, 0\n\t" \
  "v_mov_b32 v120, 0\n\t" \
  "v_mov_b32 v121, 0\n\t" \
  "v_mov_b32 v122, 0\n\t" \
  "v_mov_b32 v123, 0\n\t" \
  "v_mov_b32 v124, 0\n\t" \
  "v_mov_b32 v125, 0\n\t" \
  "v_mov_b32 v126, 0\n\t" \
  "v_mov_b32 v127, 0\n\t" \
  "v_mov_b32 v176, 0\n\t" \
  "v_mov_b32 v177, 0\n\t" \
  "v_mov_b32 v178, 0\n\t" \
  "v_mov_b32 v179, 0\n\t" \
  "v_mov_b32 v180, 0\n\t" \
  "v_mov_b32 v181, 0\n\t" \
  "v_mov_b32 v182, 0\n\t" \
  "v_mov_b32 v183, 0\n\t" \
  "v_mov_b32 v184, 0\n\t" \
  "v_mov_b32 v185, 0\n\t" \
  "v_mov_b32 v186, 0\n\t" \
  "v_mov_b32 v187, 0\n\t" \
  "v_mov_b32 v188, 0\n\t" \
  "v_mov_b32 v189, 0\n\t" \
  "v_mov_b32 v190, 0\n\t" \
  "v_mov_b32 v191, 0\n\t" \
  "v_mov_b32 v248, 0\n\t" \
  "v_mov_b32 v249, 0\n\t" \
  "v_mov_b32 v250, 0\n\t" \
  "v_mov_b32 v251, 0\n\t" \
  "v_mov_b32 v252, 0\n\t" \
  "v_mov_b32 v253, 0\n\t" \
  "v_mov_b32 v254, 0\n\t" \
  "v_mov_b32 v255, 0\n\t" \
  "s_waitcnt vmcnt(8)\n\t" \
  "v_mfma_f32_32x32x16_bf16 v[128:143], v[192:195], v[32:35], 0\n\t" \
  "v_mfma_f32_32x32x16_bf16 v[128:143], v[196:199], v[36:39], v[128:143]\n\t" \
  "s_nop 7\n\t" \
  "s_nop 3\n\t" \
  "1:\n\t" \
  "v_mfma_f32_32x32x16_bf16 v[144:159], v[192:195], v[40:43], 0\n\t" \
  "v_exp_f32 v128, v128\n\t" \
  "v_exp_f32 v129, v129\n\t" \
  "v_exp_f32 v130, v130\n\t" \
  "v_exp_f32 v131, v131\n\t" \
  "global_load_dwordx4 v[240:243], v31, s[20:21] offset:0\n\t" \
  "v_mfma_f32_32x32x16_bf16 v[144:159], v[196:199], v[44:47], v[144:159]\n\t" \
  "v_exp_f32 v132, v132\n\t" \
  "v_exp_f32 v133, v133\n\t" \
  "v_exp_f32 v134, v134\n\t" \
  "v_exp_f32 v135, v135\n\t" \
  "global_load_dwordx4 v[244:247], v31, s[20:21] offset:1024\n\t" \
  "v_smfmac_f32_32x32x32_bf16 v[112:127], v[248:251], v[176:183], v30\n\t" \
  "v_exp_f32 v136, v136\n\t" \
  "v_exp_f32 v137, v137\n\t" \
  "v_exp_f32 v138, v138\n\t" \
  "v_exp_f32 v139, v139\n\t" \
  "v_smfmac_f32_32x32x32_bf16 v[112:127], v[252:255], v[184:191], v30\n\t" \
  "v_exp_f32 v140, v140\n\t" \
  "v_exp_f32 v141, v141\n\t" \
  "v_exp_f32 v142, v142\n\t" \
  "v_exp_f32 v143, v143\n\t" \
  "v_mfma_f32_32x32x16_bf16 v[160:175], v[192:195], v[48:51], 0\n\t" \
  "v_exp_f32 v144, v144\n\t" \
  "v_exp_f32 v145, v145\n\t" \
  "v_exp_f32 v146, v146\n\t" \
  "v_exp_f32 v147, v147\n\t" \
  "v_mfma_f32_32x32x16_bf16 v[160:175], v[196:199], v[52:55], v[160:175]\n\t" \
  "v_exp_f32 v148, v148\n\t" \
  "v_exp_f32 v149, v149\n\t" \
  "v_exp_f32 v150, v150\n\t" \
  "v_exp_f32 v151, v151\n\t" \
  "v_smfmac_f32_32x32x32_bf16 v[64:79], v[200:203], v[128:135], v30\n\t" \
  "v_exp_f32 v152, v152\n\t" \
  "v_exp_f32 v153, v153\n\t" \
  "v_exp_f32 v154, v154\n\t" \
  "v_exp_f32 v155, v155\n\t" \
  "v_smfmac_f32_32x32x32_bf16 v[64:79], v[204:207], v[136:143], v30\n\t" \
  "v_exp_f32 v156, v156\n\t" \
  "v_exp_f32 v157, v157\n\t" \
  "v_exp_f32 v158, v158\n\t" \
  "v_exp_f32 v159, v159\n\t" \
  "v_mfma_f32_32x32x16_bf16 v[176:191], v[192:195], v[56:59], 0\n\t" \
  "v_exp_f32 v160, v160\n\t" \
  "v_exp_f32 v161, v161\n\t" \
  "v_exp_f32 v162, v162\n\t" \
  "v_exp_f32 v163, v163\n\t" \
  "v_mfma_f32_32x32x16_bf16 v[176:191], v[196:199], v[60:63], v[176:191]\n\t" \
  "v_exp_f32 v164, v164\n\t" \
  "v_exp_f32 v165, v165\n\t" \
  "v_exp_f32 v166, v166\n\t" \
  "v_exp_f32 v167, v167\n\t" \
  "v_smfmac_f32_32x32x32_bf16 v[80:95], v[200:203], v[144:151], v30\n\t" \
  "v_exp_f32 v168, v168\n\t" \
  "v_exp_f32 v169, v169\n\t" \
  "v_exp_f32 v170, v170\n\t" \
  "v_exp_f32 v171, v171\n\t" \
  "global_load_dwordx4 v[248:251], v31, s[20:21] offset:2048\n\t" \
  "v_smfmac_f32_32x32x32_bf16 v[80:95], v[204:207], v[152:159], v30\n\t" \
  "v_exp_f32 v172, v172\n\t" \
  "v_exp_f32 v173, v173\n\t" \
  "v_exp_f32 v174, v174\n\t" \
  "v_exp_f32 v175, v175\n\t" \
  "global_load_dwordx4 v[252:255], v31, s[20:21] offset:3072\n\t" \
  "s_add_u32 s20, s20, 4096\n\t" \
  "s_addc_u32 s21, s21, 0\n\t" \
  "s_waitcnt vmcnt(8)\n\t" \
  "v_mfma_f32_32x32x16_bf16 v[128:143], v[208:211], v[32:35], 0\n\t" \
  "v_exp_f32 v176, v176\n\t" \
  "v_exp_f32 v177, v177\n\t" \
  "v_exp_f32 v178, v178\n\t" \
  "v_exp_f32 v179, v179\n\t" \
  "v_mfma_f32_32x32x16_bf16 v[128:143], v[212:215], v[36:39], v[128:143]\n\t" \
  "v_exp_f32 v180, v180\n\t" \
  "v_exp_f32 v181, v181\n\t" \
  "v_exp_f32 v182, v182\n\t" \
  "v_exp_f32 v183, v183\n\t" \
  "v_smfmac_f32_32x32x32_bf16 v[96:111], v[200:203], v[160:167], v30\n\t" \
  "v_exp_f32 v184, v184\n\t" \
  "v_exp_f32 v185, v185\n\t" \
  "v_exp_f32 v186, v186\n\t" \
  "v_exp_f32 v187, v187\n\t" \
  "v_smfmac_f32_32x32x32_bf16 v[96:111], v[204:207], v[168:175], v30\n\t" \
  "v_exp_f32 v188, v188\n\t" \
  "v_exp_f32 v189, v189\n\t" \
  "v_exp_f32 v190, v190\n\t" \
  "v_exp_f32 v191, v191\n\t" \
  "v_mfma_f32_32x32x16_bf16 v[144:159], v[208:211], v[40:43], 0\n\t" \
  "v_exp_f32 v128, v128\n\t" \
  "v_exp_f32 v129, v129\n\t" \
  "v_exp_f32 v130, v130\n\t" \
  "v_exp_f32 v131, v131\n\t" \
  "global_load_dwordx4 v[192:195], v31, s[20:21] offset:0\n\t" \
  "v_mfma_f32_32x32x16_bf16 v[144:159], v[212:215], v[44:47], v[144:159]\n\t" \
  "v_exp_f32 v132, v132\n\t" \
  "v_exp_f32 v133, v133\n\t" \
  "v_exp_f32 v134, v134\n\t" \
  "v_exp_f32 v135, v135\n\t" \
  "global_load_dwordx4 v[196:199], v31, s[20:21] offset:1024\n\t" \
  "v_smfmac_f32_32x32x32_bf16 v[112:127], v[200:203], v[176:183], v30\n\t" \
  "v_exp_f32 v136, v136\n\t" \
  "v_exp_f32 v137, v137\n\t" \
  "v_exp_f32 v138, v138\n\t" \
  "v_exp_f32 v139, v139\n\t" \
  "v_smfmac_f32_32x32x32_bf16 v[112:127], v[204:207], v[184:191], v30\n\t" \
  "v_exp_f32 v140, v140\n\t" \
  "v_exp_f32 v141, v141\n\t" \
  "v_exp_f32 v142, v142\n\t" \
  "v_exp_f32 v143, v143\n\t" \
  "v_mfma_f32_32x32x16_bf16 v[160:175], v[208:211], v[48:51], 0\n\t" \
  "v_exp_f32 v144, v144\n\t" \
  "v_exp_f32 v145, v145\n\t" \
  "v_exp_f32 v146, v146\n\t" \
  "v_exp_f32 v147, v147\n\t" \
  "v_mfma_f32_32x32x16_bf16 v[160:175], v[212:215], v[52:55], v[160:175]\n\t" \
  "v_exp_f32 v148, v148\n\t" \
  "v_exp_f32 v149, v149\n\t" \
  "v_exp_f32 v150, v150\n\t" \
  "v_exp_f32 v151, v151\n\t" \
  "v_smfmac_f32_32x32x32_bf16 v[64:79], v[216:219], v[128:135], v30\n\t" \
  "v_exp_f32 v152, v152\n\t" \
  "v_exp_f32 v153, v153\n\t" \
  "v_exp_f32 v154, v154\n\t" \
  "v_exp_f32 v155, v155\n\t" \
  "v_smfmac_f32_32x32x32_bf16 v[64:79], v[220:223], v[136:143], v30\n\t" \
  "v_exp_f32 v156, v156\n\t" \
  "v_exp_f32 v157, v157\n\t" \
  "v_exp_f32 v158, v158\n\t" \
  "v_exp_f32 v159, v159\n\t" \
  "v_mfma_f32_32x32x16_bf16 v[176:191], v[208:211], v[56:59], 0\n\t" \
  "v_exp_f32 v160, v160\n\t" \
  "v_exp_f32 v161, v161\n\t" \
  "v_exp_f32 v162, v162\n\t" \
  "v_exp_f32 v163, v163\n\t" \
  "v_mfma_f32_32x32x16_bf16 v[176:191], v[212:215], v[60:63], v[176:191]\n\t" \
  "v_exp_f32 v164, v164\n\t" \
  "v_exp_f32 v165, v165\n\t" \
  "v_exp_f32 v166, v166\n\t" \
  "v_exp_f32 v167, v167\n\t" \
  "v_smfmac_f32_32x32x32_bf16 v[80:95], v[216:219], v[144:151], v30\n\t" \
  "v_exp_f32 v168, v168\n\t" \
  "v_exp_f32 v169, v169\n\t" \
  "v_exp_f32 v170, v170\n\t" \
  "v_exp_f32 v171, v171\n\t" \
  "global_load_dwordx4 v[200:203], v31, s[20:21] offset:2048\n\t" \
  "v_smfmac_f32_32x32x32_bf16 v[80:95], v[220:223], v[152:159], v30\n\t" \
  "v_exp_f32 v172, v172\n\t" \
  "v_exp_f32 v173, v173\n\t" \
  "v_exp_f32 v174, v174\n\t" \
  "v_exp_f32 v175, v175\n\t" \
  "global_load_dwordx4 v[204:207], v31, s[20:21] offset:3072\n\t" \
  "s_add_u32 s20, s20, 4096\n\t" \
  "s_addc_u32 s21, s21, 0\n\t" \
  "s_waitcnt vmcnt(8)\n\t" \
  "v_mfma_f32_32x32x16_bf16 v[128:143], v[224:227], v[32:35], 0\n\t" \
  "v_exp_f32 v176, v176\n\t" \
  "v_exp_f32 v177, v177\n\t" \
  "v_exp_f32 v178, v178\n\t" \
  "v_exp_f32 v179, v179\n\t" \
  "v_mfma_f32_32x32x16_bf16 v[128:143], v[228:231], v[36:39], v[128:143]\n\t" \
  "v_exp_f32 v180, v180\n\t" \
  "v_exp_f32 v181, v181\n\t" \
  "v_exp_f32 v182, v182\n\t" \
  "v_exp_f32 v183, v183\n\t" \
  "v_smfmac_f32_32x32x32_bf16 v[96:111], v[216:219], v[160:167], v30\n\t" \
  "v_exp_f32 v184, v184\n\t" \
  "v_exp_f32 v185, v185\n\t" \
  "v_exp_f32 v186, v186\n\t" \
  "v_exp_f32 v187, v187\n\t" \
  "v_smfmac_f32_32x32x32_bf16 v[96:111], v[220:223], v[168:175], v30\n\t" \
  "v_exp_f32 v188, v188\n\t" \
  "v_exp_f32 v189, v189\n\t" \
  "v_exp_f32 v190, v190\n\t" \
  "v_exp_f32 v191, v191\n\t" \
  "v_mfma_f32_32x32x16_bf16 v[144:159], v[224:227], v[40:43], 0\n\t" \
  "v_exp_f32 v128, v128\n\t" \
  "v_exp_f32 v129, v129\n\t" \
  "v_exp_f32 v130, v130\n\t" \
  "v_exp_f32 v131, v131\n\t" \
  "global_load_dwordx4 v[208:211], v31, s[20:21] offset:0\n\t" \
  "v_mfma_f32_32x32x16_bf16 v[144:159], v[228:231], v[44:47], v[144:159]\n\t" \
  "v_exp_f32 v132, v132\n\t" \
  "v_exp_f32 v133, v133\n\t" \
  "v_exp_f32 v134, v134\n\t" \
  "v_exp_f32 v135, v135\n\t" \
  "global_load_dwordx4 v[212:215], v31, s[20:21] offset:1024\n\t" \
  "v_smfmac_f32_32x32x32_bf16 v[112:127], v[216:219], v[176:183], v30\n\t" \
  "v_exp_f32 v136, v136\n\t" \
  "v_exp_f32 v137, v137\n\t" \
  "v_exp_f32 v138, v138\n\t" \
  "v_exp_f32 v139, v139\n\t" \
  "v_smfmac_f32_32x32x32_bf16 v[112:127], v[220:223], v[184:191], v30\n\t" \
  "v_exp_f32 v140, v140\n\t" \
  "v_exp_f32 v141, v141\n\t" \
  "v_exp_f32 v142, v142\n\t" \
  "v_exp_f32 v143, v143\n\t" \
  "v_mfma_f32_32x32x16_bf16 v[160:175], v[224:227], v[48:51], 0\n\t" \
  "v_exp_f32 v144, v144\n\t" \
  "v_exp_f32 v145, v145\n\t" \
  "v_exp_f32 v146, v146\n\t" \
  "v_exp_f32 v147, v147\n\t" \
  "v_mfma_f32_32x32x16_bf16 v[160:175], v[228:231], v[52:55], v[160:175]\n\t" \
  "v_exp_f32 v148, v148\n\t" \
  "v_exp_f32 v149, v149\n\t" \
  "v_exp_f32 v150, v150\n\t" \
  "v_exp_f32 v151, v151\n\t" \
  "v_smfmac_f32_32x32x32_bf16 v[64:79], v[232:235], v[128:135], v30\n\t" \
  "v_exp_f32 v152, v152\n\t" \
  "v_exp_f32 v153, v153\n\t" \
  "v_exp_f32 v154, v154\n\t" \
  "v_exp_f32 v155, v155\n\t" \
  "v_smfmac_f32_32x32x32_bf16 v[64:79], v[236:239], v[136:143], v30\n\t" \
  "v_exp_f32 v156, v156\n\t" \
  "v_exp_f32 v157, v157\n\t" \
  "v_exp_f32 v158, v158\n\t" \
  "v_exp_f32 v159, v159\n\t" \
  "v_mfma_f32_32x32x16_bf16 v[176:191], v[224:227], v[56:59], 0\n\t" \
  "v_exp_f32 v160, v160\n\t" \
  "v_exp_f32 v161, v161\n\t" \
  "v_exp_f32 v162, v162\n\t" \
  "v_exp_f32 v163, v163\n\t" \
  "v_mfma_f32_32x32x16_bf16 v[176:191], v[228:231], v[60:63], v[176:191]\n\t" \
  "v_exp_f32 v164, v164\n\t" \
  "v_exp_f32 v165, v165\n\t" \
  "v_exp_f32 v166, v166\n\t" \
  "v_exp_f32 v167, v167\n\t" \
  "v_smfmac_f32_32x32x32_bf16 v[80:95], v[232:235], v[144:151], v30\n\t" \
  "v_exp_f32 v168, v168\n\t" \
  "v_exp_f32 v169, v169\n\t" \
  "v_exp_f32 v170, v170\n\t" \
  "v_exp_f32 v171, v171\n\t" \
  "global_load_dwordx4 v[216:219], v31, s[20:21] offset:2048\n\t" \
  "v_smfmac_f32_32x32x32_bf16 v[80:95], v[236:239], v[152:159], v30\n\t" \
  "v_exp_f32 v172, v172\n\t" \
  "v_exp_f32 v173, v173\n\t" \
  "v_exp_f32 v174, v174\n\t" \
  "v_exp_f32 v175, v175\n\t" \
  "global_load_dwordx4 v[220:223], v31, s[20:21] offset:3072\n\t" \
  "s_add_u32 s20, s20, 4096\n\t" \
  "s_addc_u32 s21, s21, 0\n\t" \
  "s_waitcnt vmcnt(8)\n\t" \
  "v_mfma_f32_32x32x16_bf16 v[128:143], v[240:243], v[32:35], 0\n\t" \
  "v_exp_f32 v176, v176\n\t" \
  "v_exp_f32 v177, v177\n\t" \
  "v_exp_f32 v178, v178\n\t" \
  "v_exp_f32 v179, v179\n\t" \
  "v_mfma_f32_32x32x16_bf16 v[128:143], v[244:247], v[36:39], v[128:143]\n\t" \
  "v_exp_f32 v180, v180\n\t" \
  "v_exp_f32 v181, v181\n\t" \
  "v_exp_f32 v182, v182\n\t" \
  "v_exp_f32 v183, v183\n\t" \
  "v_smfmac_f32_32x32x32_bf16 v[96:111], v[232:235], v[160:167], v30\n\t" \
  "v_exp_f32 v184, v184\n\t" \
  "v_exp_f32 v185, v185\n\t" \
  "v_exp_f32 v186, v186\n\t" \
  "v_exp_f32 v187, v187\n\t" \
  "v_smfmac_f32_32x32x32_bf16 v[96:111], v[236:239], v[168:175], v30\n\t" \
  "v_exp_f32 v188, v188\n\t" \
  "v_exp_f32 v189, v189\n\t" \
  "v_exp_f32 v190, v190\n\t" \
  "v_exp_f32 v191, v191\n\t" \
  "v_mfma_f32_32x32x16_bf16 v[144:159], v[240:243], v[40:43], 0\n\t" \
  "v_exp_f32 v128, v128\n\t" \
  "v_exp_f32 v129, v129\n\t" \
  "v_exp_f32 v130, v130\n\t" \
  "v_exp_f32 v131, v131\n\t" \
  "global_load_dwordx4 v[224:227], v31, s[20:21] offset:0\n\t" \
  "v_mfma_f32_32x32x16_bf16 v[144:159], v[244:247], v[44:47], v[144:159]\n\t" \
  "v_exp_f32 v132, v132\n\t" \
  "v_exp_f32 v133, v133\n\t" \
  "v_exp_f32 v134, v134\n\t" \
  "v_exp_f32 v135, v135\n\t" \
  "global_load_dwordx4 v[228:231], v31, s[20:21] offset:1024\n\t" \
  "v_smfmac_f32_32x32x32_bf16 v[112:127], v[232:235], v[176:183], v30\n\t" \
  "v_exp_f32 v136, v136\n\t" \
  "v_exp_f32 v137, v137\n\t" \
  "v_exp_f32 v138, v138\n\t" \
  "v_exp_f32 v139, v139\n\t" \
  "v_smfmac_f32_32x32x32_bf16 v[112:127], v[236:239], v[184:191], v30\n\t" \
  "v_exp_f32 v140, v140\n\t" \
  "v_exp_f32 v141, v141\n\t" \
  "v_exp_f32 v142, v142\n\t" \
  "v_exp_f32 v143, v143\n\t" \
  "v_mfma_f32_32x32x16_bf16 v[160:175], v[240:243], v[48:51], 0\n\t" \
  "v_exp_f32 v144, v144\n\t" \
  "v_exp_f32 v145, v145\n\t" \
  "v_exp_f32 v146, v146\n\t" \
  "v_exp_f32 v147, v147\n\t" \
  "v_mfma_f32_32x32x16_bf16 v[160:175], v[244:247], v[52:55], v[160:175]\n\t" \
  "v_exp_f32 v148, v148\n\t" \
  "v_exp_f32 v149, v149\n\t" \
  "v_exp_f32 v150, v150\n\t" \
  "v_exp_f32 v151, v151\n\t" \
  "v_smfmac_f32_32x32x32_bf16 v[64:79], v[248:251], v[128:135], v30\n\t" \
  "v_exp_f32 v152, v152\n\t" \
  "v_exp_f32 v153, v153\n\t" \
  "v_exp_f32 v154, v154\n\t" \
  "v_exp_f32 v155, v155\n\t" \
  "v_smfmac_f32_32x32x32_bf16 v[64:79], v[252:255], v[136:143], v30\n\t" \
  "v_exp_f32 v156, v156\n\t" \
  "v_exp_f32 v157, v157\n\t" \
  "v_exp_f32 v158, v158\n\t" \
  "v_exp_f32 v159, v159\n\t" \
  "v_mfma_f32_32x32x16_bf16 v[176:191], v[240:243], v[56:59], 0\n\t" \
  "v_exp_f32 v160, v160\n\t" \
  "v_exp_f32 v161, v161\n\t" \
  "v_exp_f32 v162, v162\n\t" \
  "v_exp_f32 v163, v163\n\t" \
  "v_mfma_f32_32x32x16_bf16 v[176:191], v[244:247], v[60:63], v[176:191]\n\t" \
  "v_exp_f32 v164, v164\n\t" \
  "v_exp_f32 v165, v165\n\t" \
  "v_exp_f32 v166, v166\n\t" \
  "v_exp_f32 v167, v167\n\t" \
  "v_smfmac_f32_32x32x32_bf16 v[80:95], v[248:251], v[144:151], v30\n\t" \
  "v_exp_f32 v168, v168\n\t" \
  "v_exp_f32 v169, v169\n\t" \
  "v_exp_f32 v170, v170\n\t" \
  "v_exp_f32 v171, v171\n\t" \
  "global_load_dwordx4 v[232:235], v31, s[20:21] offset:2048\n\t" \
  "v_smfmac_f32_32x32x32_bf16 v[80:95], v[252:255], v[152:159], v30\n\t" \
  "v_exp_f32 v172, v172\n\t" \
  "v_exp_f32 v173, v173\n\t" \
  "v_exp_f32 v174, v174\n\t" \
  "v_exp_f32 v175, v175\n\t" \
  "global_load_dwordx4 v[236:239], v31, s[20:21] offset:3072\n\t" \
  "s_add_u32 s20, s20, 4096\n\t" \
  "s_addc_u32 s21, s21, 0\n\t" \
  "s_waitcnt vmcnt(8)\n\t" \
  "v_mfma_f32_32x32x16_bf16 v[128:143], v[192:195], v[32:35], 0\n\t" \
  "v_exp_f32 v176, v176\n\t" \
  "v_exp_f32 v177, v177\n\t" \
  "v_exp_f32 v178, v178\n\t" \
  "v_exp_f32 v179, v179\n\t" \
  "v_mfma_f32_32x32x16_bf16 v[128:143], v[196:199], v[36:39], v[128:143]\n\t" \
  "v_exp_f32 v180, v180\n\t" \
  "v_exp_f32 v181, v181\n\t" \
  "v_exp_f32 v182, v182\n\t" \
  "v_exp_f32 v183, v183\n\t" \
  "v_smfmac_f32_32x32x32_bf16 v[96:111], v[248:251], v[160:167], v30\n\t" \
  "v_exp_f32 v184, v184\n\t" \
  "v_exp_f32 v185, v185\n\t" \
  "v_exp_f32 v186, v186\n\t" \
  "v_exp_f32 v187, v187\n\t" \
  "v_smfmac_f32_32x32x32_bf16 v[96:111], v[252:255], v[168:175], v30\n\t" \
  "v_exp_f32 v188, v188\n\t" \
  "v_exp_f32 v189, v189\n\t" \
  "v_exp_f32 v190, v190\n\t" \
  "v_exp_f32 v191, v191\n\t" \
  "s_sub_u32 s22, s22, 1\n\t" \
  "s_cmp_lg_u32 s22, 0\n\t" \
  "s_cbranch_scc1 1b\n\t" \
  "s_nop 1\n\t" \
  "v_smfmac_f32_32x32x32_bf16 v[112:127], v[248:251], v[176:183], v30\n\t" \
  "v_smfmac_f32_32x32x32_bf16 v[112:127], v[252:255], v[184:191], v30\n\t" \
  "s_waitcnt vmcnt(0)\n\t" \
  "s_nop 15\n\t"
#define ATTN_CLOB "v30", "v128", "v129", "v130", "v131", "v132", "v133", "v134", "v135", "v136", "v137", "v138", "v139", "v140", "v141", "v142", "v143", "v144", "v145", "v146", "v147", "v148", "v149", "v150", "v151", "v152", "v153", "v154", "v155", "v156", "v157", "v158", "v159", "v160", "v161", "v162", "v163", "v164", "v165", "v166", "v167", "v168", "v169", "v170", "v171", "v172", "v173", "v174", "v175", "v176", "v177", "v178", "v179", "v180", "v181", "v182", "v183", "v184", "v185", "v186", "v187", "v188", "v189", "v190", "v191", "v192", "v193", "v194", "v195", "v196", "v197", "v198", "v199", "v200", "v201", "v202", "v203", "v204", "v205", "v206", "v207", "v208", "v209", "v210", "v211", "v212", "v213", "v214", "v215", "v216", "v217", "v218", "v219", "v220", "v221", "v222", "v223", "v224", "v225", "v226", "v227", "v228", "v229", "v230", "v231", "v232", "v233", "v234", "v235", "v236", "v237", "v238", "v239", "v240", "v241", "v242", "v243", "v244", "v245", "v246", "v247", "v248", "v249", "v250", "v251", "v252", "v253", "v254", "v255", "s20", "s21", "s22", "scc", "memory"
constexpr int K2_LDS = 65536 + 4096;
__global__ __launch_bounds__(256, 1) void k2(char* __restrict__ ws) {
  extern __shared__ __attribute__((aligned(16))) char smem[];
  LAS char* lds = (LAS char*)smem;
  const int tid = threadIdx.x, lane = tid & 63, w = __builtin_amdgcn_readfirstlane(tid >> 6), r32 = lane & 31, hi = lane >> 5;
  const int tb = blockIdx.x >> 4, j = blockIdx.x & 15;
  const char* qp = ws + WS_Q + (size_t)(tb * 16 + j) * 8192 + lane * 16;
  u32x8 q0, q1, q2, q3;
  { u32x4 t[8];
#pragma unroll
    for (int i = 0; i < 8; ++i) t[i] = *(const u32x4*)(qp + i * 1024);
    q0 = __builtin_shufflevector(t[0], t[1], 0, 1, 2, 3, 4, 5, 6, 7); q1 = __builtin_shufflevector(t[2], t[3], 0, 1, 2, 3, 4, 5, 6, 7);
    q2 = __builtin_shufflevector(t[4], t[5], 0, 1, 2, 3, 4, 5, 6, 7); q3 = __builtin_shufflevector(t[6], t[7], 0, 1, 2, 3, 4, 5, 6, 7); }
  const char* kvw = ws + WS_KV + (size_t)w * 16 * 4096;
  f32x16 o[4];
  asm volatile(ATTN_ASM : "={v[64:79]}"(o[0]), "={v[80:95]}"(o[1]), "={v[96:111]}"(o[2]), "={v[112:127]}"(o[3])
               : "{v[32:39]}"(q0), "{v[40:47]}"(q1), "{v[48:55]}"(q2), "{v[56:63]}"(q3), "{v31}"(lane * 16), [kv] "s"(kvw) : ATTN_CLOB);
  LAS int* flag = (LAS int*)(lds + 65536);
  f32x16 t; float l;
  for (int pass = 0;; ++pass) {
    if (tid == 0) flag[0] = 0;
#pragma unroll
    for (int b = 0; b < 4; ++b)
#pragma unroll
      for (int g = 0; g < 4; ++g) *(LAS f32x4*)(lds + ((w * 4 + b) * 4 + g) * 1024 + lane * 16) = (f32x4){o[b][4 * g], o[b][4 * g + 1], o[b][4 * g + 2], o[b][4 * g + 3]};
    __syncthreads();
#pragma unroll
    for (int g = 0; g < 4; ++g) { f32x4 a = *(LAS f32x4*)(lds + ((0 * 4 + w) * 4 + g) * 1024 + lane * 16);
#pragma unroll
      for (int v = 1; v < 4; ++v) a += *(LAS f32x4*)(lds + ((v * 4 + w) * 4 + g) * 1024 + lane * 16);
#pragma unroll
      for (int i = 0; i < 4; ++i) t[4 * g + i] = a[i]; }
    l = swapadd(hi ? t[12] : 0.f);
    if (pass) break;
    if (!(l >= 0x1p-60f && l <= 0x1p60f)) flag[0] = 1;
    __syncthreads();
    const int bad = flag[0];
    __syncthreads();
    if (!bad) break;
    const u32x8 qq[4] = {q0, q1, q2, q3};
    float m[4] = {-INFINITY, -INFINITY, -INFINITY, -INFINITY};
    LAS float* mx = (LAS float*)(lds + 65536 + 64);
    for (int ph = 0; ph < 2; ++ph) {
      for (int kt = 0; kt < 16; ++kt) {
        const char* p = kvw + kt * 4096 + lane * 16;
        const bf16x8 k0 = *(const bf16x8*)p, k1 = *(const bf16x8*)(p + 1024); const u32x4 v0 = *(const u32x4*)(p + 2048), v1 = *(const u32x4*)(p + 3072);
#pragma unroll
        for (int b = 0; b < 4; ++b) {
          f32x16 s = MFMA(k0, __builtin_bit_cast(bf16x8, __builtin_shufflevector(qq[b], qq[b], 0, 1, 2, 3)), f32x16{});
          s = MFMA(k1, __builtin_bit_cast(bf16x8, __builtin_shufflevector(qq[b], qq[b], 4, 5, 6, 7)), s);
          if (ph == 0) {
#pragma unroll
            for (int i = 0; i < 16; ++i) m[b] = fmaxf(m[b], s[i]);
          } else {
            f32x8 p0, p1;
#pragma unroll
            for (int i = 0; i < 8; ++i) { p0[i] = __builtin_amdgcn_exp2f(s[i] - m[b]); p1[i] = __builtin_amdgcn_exp2f(s[i + 8] - m[b]); }
            asm volatile("s_nop 1\n\tv_smfmac_f32_32x32x32_bf16 %0, %1, %2, %5\n\tv_smfmac_f32_32x32x32_bf16 %0, %3, %4, %5\n\ts_nop 15" : "+v"(o[b]) : "v"(v0), "v"(p0), "v"(v1), "v"(p1), "v"(0xddddddddu));
          }
        }
      }
      if (ph == 0) {
#pragma unroll
        for (int b = 0; b < 4; ++b) { m[b] = swapmax(m[b]); mx[(w * 4 + b) * 32 + r32] = m[b]; o[b] = f32x16{}; }
        __syncthreads();
#pragma unroll
        for (int b = 0; b < 4; ++b) m[b] = fmaxf(fmaxf(mx[b * 32 + r32], mx[(4 + b) * 32 + r32]), fmaxf(mx[(8 + b) * 32 + r32], mx[(12 + b) * 32 + r32]));
      }
    }
  }
  t *= __builtin_amdgcn_rcpf(l);
  char* ho = ws + WS_HO + ((size_t)(tb * 4 + w) * NKS + 112 + 2 * j) * 1024 + lane * 16;
  *(u32x4*)ho = (u32x4){cvtpk(t[0], t[1]), cvtpk(t[2], t[3]), cvtpk(t[4], t[5]), cvtpk(t[6], t[7])};
  *(u32x4*)(ho + 1024) = (u32x4){cvtpk(t[8], t[9]), cvtpk(t[10], t[11]), hi ? 0u : cvtpk(t[12], t[13]), hi ? 0u : cvtpk(t[14], t[15])};
}

constexpr int K3_RING = 8;
constexpr int K3_LDS = 4 * 64 * 272;
__global__ __launch_bounds__(256, 1) void k3(const char* __restrict__ x, const char* __restrict__ wd, const char* __restrict__ wo, const char* __restrict__ ws, char* __restrict__ out) {
  extern __shared__ __attribute__((aligned(16))) char smem[];
  LAS char* lds = (LAS char*)smem;
  const unsigned lds0 = (unsigned)(uintptr_t)lds;
  const int tid = threadIdx.x, lane = tid & 63, w = __builtin_amdgcn_readfirstlane(tid >> 6), r32 = lane & 31, hi = lane >> 5;
  const int xcd = blockIdx.x & 7, ii = blockIdx.x >> 3, tm = xcd * 4 + ii / 7, tn = ii % 7;
  const int drow = lane >> 3, dch = (lane & 7) ^ (((drow >> 1) & 1) << 2);
  const char* wdp = wd + (size_t)(448 * w + drow) * 896 + tn * 128 + dch * 16;
  const char* wop = wo + tn * 128 + dch * 16;
  const char* hp = ws + WS_HO + (size_t)(tm * 2) * NKS * 1024 + lane * 16;
  const unsigned ring = lds0 + w * K3_RING * 2048;
  u32x4 fb[6][2];
  auto issue = [&](int i) {
    if (i < 28) { glds16(wdp + (size_t)i * 16 * 896, ring + (i % K3_RING) * 2048); glds16(wdp + (size_t)(i * 16 + 8) * 896, ring + (i % K3_RING) * 2048 + 1024); }
    else { const int r0 = (4 * w + ((i - 28) >> 1)) * 28 + ((i - 28) & 1) * 16 + drow;
      glds16(wop + (size_t)r0 * 896, ring + (i % K3_RING) * 2048); glds16(wop + (size_t)min(r0 + 8, 447) * 896, ring + (i % K3_RING) * 2048 + 1024); }
    const int ks = i < 28 ? 28 * w + i : 112 + 8 * w + (i - 28);
    asm volatile("global_load_dwordx4 %0, %1, off" : "=v"(fb[i % 6][0]) : "v"(hp + (size_t)ks * 1024) : "memory");
    asm volatile("global_load_dwordx4 %0, %1, off" : "=v"(fb[i % 6][1]) : "v"(hp + (size_t)(ks + NKS) * 1024) : "memory");
  };
  const int q = (lane & 15) >> 2;
  const int aoff = w * K3_RING * 2048 + (4 * hi + q) * 128 + ((lane >> 4) & 1) * 32 + (lane & 3) * 8, sw = ((q >> 1) & 1) * 64;
  f32x16 acc[2][2] = {};
#pragma unroll
  for (int i = 0; i < 5; ++i) issue(i);
#pragma unroll
  for (int i = 0; i < 36; ++i) {
    if (i + 5 < 36) issue(i + 5);
    constexpr int dummy = 0; (void)dummy;
    const int left = (36 - 1 - i) < 5 ? (36 - 1 - i) : 5;
    switch (left) {
      case 5: asm volatile("s_waitcnt vmcnt(20)" : "+v"(fb[i % 6][0]), "+v"(fb[i % 6][1]) :: "memory"); break;
      case 4: asm volatile("s_waitcnt vmcnt(16)" : "+v"(fb[i % 6][0]), "+v"(fb[i % 6][1]) :: "memory"); break;
      case 3: asm volatile("s_waitcnt vmcnt(12)" : "+v"(fb[i % 6][0]), "+v"(fb[i % 6][1]) :: "memory"); break;
      case 2: asm volatile("s_waitcnt vmcnt(8)" : "+v"(fb[i % 6][0]), "+v"(fb[i % 6][1]) :: "memory"); break;
      case 1: asm volatile("s_waitcnt vmcnt(4)" : "+v"(fb[i % 6][0]), "+v"(fb[i % 6][1]) :: "memory"); break;
      default: asm volatile("s_waitcnt vmcnt(0)" : "+v"(fb[i % 6][0]), "+v"(fb[i % 6][1]) :: "memory"); break;
    }
    LAS const char* p = lds + aoff + (i % K3_RING) * 2048;
    const bf16x8 a0 = cat(trrd(p + sw), trrd(p + sw + 1024)), a1 = cat(trrd(p + (64 ^ sw)), trrd(p + (64 ^ sw) + 1024));
    const bf16x8 b0 = __builtin_bit_cast(bf16x8, fb[i % 6][0]), b1 = __builtin_bit_cast(bf16x8, fb[i % 6][1]);
    acc[0][0] = MFMA(a0, b0, acc[0][0]); acc[0][1] = MFMA(a0, b1, acc[0][1]); acc[1][0] = MFMA(a1, b0, acc[1][0]); acc[1][1] = MFMA(a1, b1, acc[1][1]);
    SBAR();
  }
  __syncthreads();
#pragma unroll
  for (int t = 0; t < 2; ++t)
#pragma unroll
    for (int b = 0; b < 2; ++b)
#pragma unroll
      for (int g = 0; g < 4; ++g)
        *(LAS f32x4*)(lds + (w * 64 + 32 * b + r32) * 272 + (32 * t + 8 * g + 4 * hi) * 4) = (f32x4){acc[t][b][4 * g], acc[t][b][4 * g + 1], acc[t][b][4 * g + 2], acc[t][b][4 * g + 3]};
  __syncthreads();
#pragma unroll
  for (int it = 0; it < 2; ++it) {
    const int row = it * 32 + (tid >> 3), c8 = tid & 7;
    const size_t go = (size_t)(tm * 64 + row) * 896 + tn * 128 + c8 * 16;
    const u32x4 xv = *(const u32x4*)(x + go);
    f32x4 a = {}, b = {};
#pragma unroll
    for (int v = 0; v < 4; ++v) { a += *(LAS f32x4*)(lds + (v * 64 + row) * 272 + c8 * 32); b += *(LAS f32x4*)(lds + (v * 64 + row) * 272 + c8 * 32 + 16); }
    u32x4 r;
    r[0] = cvtpk(a[0] + __uint_as_float(xv[0] << 16), a[1] + __uint_as_float(xv[0] & 0xffff0000u));
    r[1] = cvtpk(a[2] + __uint_as_float(xv[1] << 16), a[3] + __uint_as_float(xv[1] & 0xffff0000u));
    r[2] = cvtpk(b[0] + __uint_as_float(xv[2] << 16), b[1] + __uint_as_float(xv[2] & 0xffff0000u));
    r[3] = cvtpk(b[2] + __uint_as_float(xv[3] << 16), b[3] + __uint_as_float(xv[3] & 0xffff0000u));
    *(u32x4*)(out + go) = r;
  }
}

extern "C" void kernel_launch(void* const* d_in, const int* in_sizes, int n_in, void* d_out, int out_size, void* d_ws, size_t ws_size, hipStream_t stream) {
  const char* x = (const char*)d_in[0];
  (void)hipFuncSetAttribute((const void*)k1, hipFuncAttributeMaxDynamicSharedMemorySize, K1_LDS);
  (void)hipFuncSetAttribute((const void*)k2, hipFuncAttributeMaxDynamicSharedMemorySize, K2_LDS);
  (void)hipFuncSetAttribute((const void*)k3, hipFuncAttributeMaxDynamicSharedMemorySize, K3_LDS);
  k1<<<256, 256, K1_LDS, stream>>>(x, (const char*)d_in[1], (const char*)d_in[2], (const char*)d_in[3], (const char*)d_in[6], (const char*)d_in[7], (char*)d_ws);
  k2<<<256, 256, K2_LDS, stream>>>((char*)d_ws);
  k3<<<224, 256, K3_LDS, stream>>>(x, (const char*)d_in[8], (const char*)d_in[4], (const char*)d_ws, (char*)d_out);
}
```

```cpp
#include <hip/hip_runtime.h>
#include <stdint.h>

#define FUSED_ASM \
  ".Lfz_start:\n" \
    \
  "s_load_dwordx16 s[4:19], s[0:1], 0\n" \
  "s_load_dwordx16 s[20:35], s[0:1], 64\n" \
  "s_load_dwordx8 s[36:43], s[0:1], 128\n" \
    \
  "s_getpc_b64 s[54:55]\n" \
  ".Lfz_pc:\n" \
  "v_lshrrev_b32 v29, 6, v0\n" \
  "s_and_b32 s53, s2, 0xf8\n" \
  "v_add_lshl_u32 v29, v29, s53, 7\n" \
  "v_min_u32 v29, .Lfz_end-.Lfz_pc-4, v29\n" \
  "global_load_dword v247, v29, s[54:55]\n" \
  "v_readfirstlane_b32 s44, v0\n" \
  "s_and_b32 s47, s2, 1\n" \
  "s_bfe_u32 s53, s2, 0x20001\n" \
  "s_bfe_u32 s54, s2, 0x20003\n" \
  "s_lshr_b32 s48, s2, 5\n" \
  "s_lshl2_add_u32 s46, s53, s54\n" \
  "s_lshr_b32 s44, s44, 6\n" \
  "s_and_b32 s45, s44, 3\n" \
  "s_lshl1_add_u32 s52, s53, s47\n" \
  "s_lshl2_add_u32 s52, s52, s54\n" \
  "s_mov_b32 s53, 0xa965432b\n" \
  "s_cmp_eq_u32 s47, 0\n" \
  "s_cselect_b32 s53, s53, 0xfe87c10d\n" \
  "s_lshl_b32 s54, s48, 2\n" \
  "s_lshr_b32 s53, s53, s54\n" \
  "s_and_b32 s49, s53, 15\n" \
  "s_sub_u32 s48, s48, 1\n" \
  "s_cselect_b32 s63, 8, 0\n" \
  "s_mul_i32 s50, s47, 7\n" \
  "s_add_u32 s50, s50, s48\n" \
  "s_mov_b32 s62, 0x07060302\n" \
  "s_add_u32 s63, s63, s44\n" \
  "s_cmp_ge_u32 s63, 12\n" \
  "s_cbranch_scc1 .Lfz_SG\n" \
  "s_cmp_ge_u32 s63, 8\n" \
  "s_cbranch_scc1 .Lfz_SA\n" \
  "s_cmp_ge_u32 s63, 4\n" \
  "s_cbranch_scc1 .Lfz_G\n" \
    \
  "v_bfe_u32 v25, v0, 3, 3\n" \
  "v_mul_u32_u24 v25, 896, v25\n" \
  "v_bfe_u32 v23, v0, 4, 2\n" \
  "v_and_b32 v24, 7, v0\n" \
  "v_xor_b32 v24, v24, v23\n" \
  "v_lshl_add_u32 v25, v24, 4, v25\n" \
  "s_lshl2_add_u32 s55, s46, s45\n" \
  "s_mul_i32 s55, s55, 28672\n" \
  "s_lshl_b32 s59, s45, 12\n" \
  "v_mul_u32_u24 v22, 3584, v23\n" \
  "v_and_b32 v24, 15, v0\n" \
  "v_lshlrev_b32 v23, 2, v23\n" \
  "v_xor_b32 v24, v24, v23\n" \
  "v_lshl_add_u32 v22, v24, 4, v22\n" \
  "v_bfe_u32 v240, v0, 2, 4\n" \
  "v_mul_u32_u24 v240, 896, v240\n" \
  "v_and_b32 v24, 3, v0\n" \
  "v_lshl_add_u32 v240, v24, 4, v240\n" \
  "s_mul_i32 s57, s49, 56\n" \
  "s_and_b32 s57, s57, 0xfffffff0\n" \
  "s_mul_i32 s53, s45, 14336\n" \
  "s_add_u32 s57, s57, s53\n" \
  "s_mul_i32 s56, s45, 57344\n" \
  "s_lshl_b32 s53, s50, 8\n" \
  "s_add_u32 s56, s56, s53\n" \
  "s_lshl_b32 s60, s45, 10\n" \
  "s_add_u32 s60, s60, 49152\n" \
  "s_add_u32 s58, s46, s48\n" \
  "s_and_b32 s58, s58, 3\n" \
  "s_and_b32 s94, s2, 6\n" \
  "s_lshl_b32 s84, s94, 7\n" \
  "s_add_u32 s84, s84, s55\n" \
  "s_mul_i32 s85, s94, 229376\n" \
  "s_add_u32 s85, s85, s56\n" \
  "s_mul_i32 s86, s94, 57344\n" \
  "s_add_u32 s86, s86, s57\n" \
  "s_xor_b32 s53, s58, 0\n" \
  "s_lshl_b32 s54, s53, 10\n" \
  "s_add_u32 s96, s54, s59\n" \
  "s_lshl_b32 s54, s53, 6\n" \
  "s_and_b32 s54, s54, 64\n" \
  "v_xor_b32 v224, s54, v25\n" \
  "s_lshl_b32 s54, s53, 4\n" \
  "s_and_b32 s54, s54, 32\n" \
  "v_xor_b32 v228, s54, v22\n" \
  "s_mul_i32 s53, s53, 7168\n" \
  "v_add_u32 v224, s53, v224\n" \
  "s_lshl_b32 s53, s53, 1\n" \
  "v_add_u32 v228, s53, v228\n" \
  "s_xor_b32 s53, s58, 1\n" \
  "s_lshl_b32 s54, s53, 10\n" \
  "s_add_u32 s97, s54, s59\n" \
  "s_lshl_b32 s54, s53, 6\n" \
  "s_and_b32 s54, s54, 64\n" \
  "v_xor_b32 v225, s54, v25\n" \
  "s_lshl_b32 s54, s53, 4\n" \
  "s_and_b32 s54, s54, 32\n" \
  "v_xor_b32 v229, s54, v22\n" \
  "s_mul_i32 s53, s53, 7168\n" \
  "v_add_u32 v225, s53, v225\n" \
  "s_lshl_b32 s53, s53, 1\n" \
  "v_add_u32 v229, s53, v229\n" \
  "s_xor_b32 s53, s58, 2\n" \
  "s_lshl_b32 s54, s53, 10\n" \
  "s_add_u32 s98, s54, s59\n" \
  "s_lshl_b32 s54, s53, 6\n" \
  "s_and_b32 s54, s54, 64\n" \
  "v_xor_b32 v226, s54, v25\n" \
  "s_lshl_b32 s54, s53, 4\n" \
  "s_and_b32 s54, s54, 32\n" \
  "v_xor_b32 v230, s54, v22\n" \
  "s_mul_i32 s53, s53, 7168\n" \
  "v_add_u32 v226, s53, v226\n" \
  "s_lshl_b32 s53, s53, 1\n" \
  "v_add_u32 v230, s53, v230\n" \
  "s_xor_b32 s53, s58, 3\n" \
  "s_lshl_b32 s54, s53, 10\n" \
  "s_add_u32 s99, s54, s59\n" \
  "s_lshl_b32 s54, s53, 6\n" \
  "s_and_b32 s54, s54, 64\n" \
  "v_xor_b32 v227, s54, v25\n" \
  "s_lshl_b32 s54, s53, 4\n" \
  "s_and_b32 s54, s54, 32\n" \
  "v_xor_b32 v231, s54, v22\n" \
  "s_mul_i32 s53, s53, 7168\n" \
  "v_add_u32 v227, s53, v227\n" \
  "s_lshl_b32 s53, s53, 1\n" \
  "v_add_u32 v231, s53, v231\n" \
  "v_and_b32 v1, 63, v0\n" \
  "v_lshlrev_b32 v2, 4, v1\n" \
  "v_and_b32 v3, 31, v0\n" \
  "v_bfe_u32 v4, v0, 5, 1\n" \
  "v_bfe_u32 v5, v0, 2, 2\n" \
  "v_bfe_u32 v7, v0, 4, 1\n" \
  "v_and_b32 v6, 3, v0\n" \
  "v_lshl_add_u32 v6, v7, 2, v6\n" \
  "v_bfe_u32 v7, v0, 1, 3\n" \
  "v_xor_b32 v7, v7, v4\n" \
  "v_lshlrev_b32 v7, 4, v7\n" \
  "v_lshl_add_u32 v7, v3, 7, v7\n" \
  "s_waitcnt lgkmcnt(0)\n" \
  "s_add_u32 m0, s96, 0\n" \
  "s_nop 0\n" \
  "buffer_load_dwordx4 v224, s[4:7], s84 offen lds\n" \
  "s_add_u32 m0, s96, 16384\n" \
  "s_nop 0\n" \
  "buffer_load_dwordx4 v228, s[24:27], s85 offen lds\n" \
  "s_add_u32 m0, s96, 32768\n" \
  "s_nop 0\n" \
  "buffer_load_dwordx4 v228, s[28:31], s85 offen lds\n" \
  "s_add_u32 m0, s60, 0\n" \
  "s_nop 0\n" \
  "buffer_load_dwordx4 v240, s[8:11], s86 offen lds\n" \
  "s_add_u32 s94, s94, 1\n" \
  "s_cmp_eq_u32 s94, 7\n" \
  "s_cselect_b32 s94, 0, s94\n" \
  "s_lshl_b32 s12, s94, 7\n" \
  "s_add_u32 s12, s12, s55\n" \
  "s_mul_i32 s13, s94, 229376\n" \
  "s_add_u32 s13, s13, s56\n" \
  "s_mul_i32 s14, s94, 57344\n" \
  "s_add_u32 s14, s14, s57\n" \
  "s_and_b32 s53, s49, 1\n" \
  "v_add_u32 v18, s53, v6\n" \
  "v_and_b32 v18, 7, v18\n" \
  "v_lshl_add_u32 v17, v4, 3, v5\n" \
  "v_lshlrev_b32 v17, 6, v17\n" \
  "v_lshl_add_u32 v17, v18, 3, v17\n" \
  "v_add_u32 v17, 49152, v17\n" \
  "s_lshl_b32 s53, s45, 12\n" \
  "v_add_u32 v18, s53, v7\n" \
  "v_xor_b32 v19, 32, v18\n" \
  "v_xor_b32 v20, 64, v18\n" \
  "v_xor_b32 v21, 0x60, v18\n" \
  "v_mov_b32 v196, 0x3f803f80\n" \
  "v_mov_b32 v197, v196\n" \
  "v_mov_b64 v[198:199], v[196:197]\n" \
  "s_and_b32 s54, s46, 2\n" \
  "s_add_u32 s54, s54, s45\n" \
  "s_and_b32 s53, s46, 1\n" \
  "s_lshl_b32 s93, s53, 8\n" \
  "s_add_u32 s53, s53, 1\n" \
  "s_lshl_b32 s53, s53, 14\n" \
  "v_lshl_add_u32 v241, v4, 3, v5\n" \
  "v_lshlrev_b32 v241, 8, v241\n" \
  "v_xor_b32 v242, s54, v5\n" \
  "v_lshl_add_u32 v241, v242, 6, v241\n" \
  "v_lshl_add_u32 v241, v6, 3, v241\n" \
  "v_lshlrev_b32 v242, 5, v4\n" \
  "v_xor_b32 v241, v241, v242\n" \
  "v_add_u32 v241, s53, v241\n" \
  "s_and_b32 s53, s46, 3\n" \
  "s_cmp_lt_u32 s45, 2\n" \
  "s_cselect_b32 s54, s54, s53\n" \
  "s_cselect_b32 s93, s93, 512\n" \
  "s_lshl_b32 s54, s54, 12\n" \
  "s_add_u32 s93, s93, s54\n" \
  "s_lshl_b32 s92, s50, 14\n" \
  "s_add_u32 s92, s92, 9715712\n" \
  "s_lshr_b32 s53, s46, 2\n" \
  "s_lshl_b32 s53, s53, 10\n" \
  "s_add_u32 s93, s93, s53\n" \
  "s_add_u32 s93, s93, s92\n" \
  "s_add_u32 s92, s92, s59\n" \
  "s_cmp_eq_u32 s45, 3\n" \
  "s_cselect_b32 s93, 0x7f000000, s93\n" \
  "v_lshlrev_b32 v242, 3, v3\n" \
  "v_lshlrev_b32 v243, 3, v1\n" \
  "v_mov_b64 v[64:65], 0\n" \
  "v_mov_b64 v[66:67], 0\n" \
  "v_mov_b64 v[68:69], 0\n" \
  "v_mov_b64 v[70:71], 0\n" \
  "v_mov_b64 v[72:73], 0\n" \
  "v_mov_b64 v[74:75], 0\n" \
  "v_mov_b64 v[76:77], 0\n" \
  "v_mov_b64 v[78:79], 0\n" \
  "v_mov_b64 v[80:81], 0\n" \
  "v_mov_b64 v[82:83], 0\n" \
  "v_mov_b64 v[84:85], 0\n" \
  "v_mov_b64 v[86:87], 0\n" \
  "v_mov_b64 v[88:89], 0\n" \
  "v_mov_b64 v[90:91], 0\n" \
  "v_mov_b64 v[92:93], 0\n" \
  "v_mov_b64 v[94:95], 0\n" \
  "v_mov_b64 v[96:97], 0\n" \
  "v_mov_b64 v[98:99], 0\n" \
  "v_mov_b64 v[100:101], 0\n" \
  "v_mov_b64 v[102:103], 0\n" \
  "v_mov_b64 v[104:105], 0\n" \
  "v_mov_b64 v[106:107], 0\n" \
  "v_mov_b64 v[108:109], 0\n" \
  "v_mov_b64 v[110:111], 0\n" \
  "v_mov_b64 v[112:113], 0\n" \
  "v_mov_b64 v[114:115], 0\n" \
  "v_mov_b64 v[116:117], 0\n" \
  "v_mov_b64 v[118:119], 0\n" \
  "v_mov_b64 v[120:121], 0\n" \
  "v_mov_b64 v[122:123], 0\n" \
  "v_mov_b64 v[124:125], 0\n" \
  "v_mov_b64 v[126:127], 0\n" \
  "s_add_u32 m0, s97, 0\n" \
  "s_nop 0\n" \
  "buffer_load_dwordx4 v225, s[4:7], s84 offen lds\n" \
  "s_add_u32 m0, s97, 16384\n" \
  "s_nop 0\n" \
  "buffer_load_dwordx4 v229, s[24:27], s85 offen lds\n" \
  "s_add_u32 m0, s97, 32768\n" \
  "s_nop 0\n" \
  "buffer_load_dwordx4 v229, s[28:31], s85 offen lds\n" \
  "s_add_u32 m0, s98, 0\n" \
  "s_nop 0\n" \
  "buffer_load_dwordx4 v226, s[4:7], s84 offen lds\n" \
  "s_add_u32 m0, s98, 16384\n" \
  "s_nop 0\n" \
  "buffer_load_dwordx4 v230, s[24:27], s85 offen lds\n" \
  "s_add_u32 m0, s98, 32768\n" \
  "s_nop 0\n" \
  "buffer_load_dwordx4 v230, s[28:31], s85 offen lds\n" \
  "s_add_u32 m0, s99, 0\n" \
  "s_nop 0\n" \
  "buffer_load_dwordx4 v227, s[4:7], s84 offen lds\n" \
  "s_add_u32 m0, s99, 16384\n" \
  "s_nop 0\n" \
  "buffer_load_dwordx4 v231, s[24:27], s85 offen lds\n" \
  "s_add_u32 m0, s99, 32768\n" \
  "s_nop 0\n" \
  "buffer_load_dwordx4 v231, s[28:31], s85 offen lds\n" \
  "s_add_u32 m0, s96, 53248\n" \
  "s_nop 0\n" \
  "buffer_load_dwordx4 v224, s[4:7], s12 offen lds\n" \
  "s_add_u32 m0, s96, 69632\n" \
  "s_nop 0\n" \
  "buffer_load_dwordx4 v228, s[24:27], s13 offen lds\n" \
  "s_add_u32 m0, s96, 86016\n" \
  "s_nop 0\n" \
  "buffer_load_dwordx4 v228, s[28:31], s13 offen lds\n" \
  "s_add_u32 m0, s60, 53248\n" \
  "s_nop 0\n" \
  "buffer_load_dwordx4 v240, s[8:11], s14 offen lds\n" \
  "s_add_u32 s94, s94, 1\n" \
  "s_cmp_eq_u32 s94, 7\n" \
  "s_cselect_b32 s94, 0, s94\n" \
  "s_lshl_b32 s16, s94, 7\n" \
  "s_add_u32 s16, s16, s55\n" \
  "s_mul_i32 s17, s94, 229376\n" \
  "s_add_u32 s17, s17, s56\n" \
  "s_mul_i32 s18, s94, 57344\n" \
  "s_add_u32 s18, s18, s57\n" \
  "s_waitcnt vmcnt(4)\n" \
  "s_barrier\n" \
    \
  "s_setprio 3\n" \
  "s_add_u32 m0, s97, 53248\n" \
  "s_nop 0\n" \
  "buffer_load_dwordx4 v225, s[4:7], s12 offen lds\n" \
  "s_add_u32 m0, s97, 69632\n" \
  "s_nop 0\n" \
  "buffer_load_dwordx4 v229, s[24:27], s13 offen lds\n" \
  "s_add_u32 m0, s97, 86016\n" \
  "s_nop 0\n" \
  "buffer_load_dwordx4 v229, s[28:31], s13 offen lds\n" \
  "s_add_u32 m0, s98, 53248\n" \
  "s_nop 0\n" \
  "buffer_load_dwordx4 v226, s[4:7], s12 offen lds\n" \
  "s_add_u32 m0, s98, 69632\n" \
  "s_nop 0\n" \
  "buffer_load_dwordx4 v230, s[24:27], s13 offen lds\n" \
  "s_add_u32 m0, s98, 86016\n" \
  "s_nop 0\n" \
  "buffer_load_dwordx4 v230, s[28:31], s13 offen lds\n" \
  "s_add_u32 m0, s99, 53248\n" \
  "s_nop 0\n" \
  "buffer_load_dwordx4 v227, s[4:7], s12 offen lds\n" \
  "s_add_u32 m0, s99, 69632\n" \
  "s_nop 0\n" \
  "buffer_load_dwordx4 v231, s[24:27], s13 offen lds\n" \
  "s_add_u32 m0, s99, 86016\n" \
  "s_nop 0\n" \
  "buffer_load_dwordx4 v231, s[28:31], s13 offen lds\n" \
  "v_mov_b32 v244, 9981952\n" \
  "buffer_load_dword v244, v244, s[36:39], 0 offen sc1\n" \
  "ds_read_b64_tr_b16 v[32:33], v17 offset:0\n" \
  "ds_read_b64_tr_b16 v[34:35], v17 offset:256\n" \
  "ds_read_b128 v[36:39], v18\n" \
  "ds_read_b64_tr_b16 v[40:41], v17 offset:1024\n" \
  "ds_read_b64_tr_b16 v[42:43], v17 offset:1280\n" \
  "ds_read_b128 v[44:47], v19\n" \
  "ds_read_b64_tr_b16 v[48:49], v17 offset:2048\n" \
  "ds_read_b64_tr_b16 v[50:51], v17 offset:2304\n" \
  "ds_read_b128 v[52:55], v20\n" \
  "ds_read_b64_tr_b16 v[56:57], v17 offset:3072\n" \
  "ds_read_b64_tr_b16 v[58:59], v17 offset:3328\n" \
  "ds_read_b128 v[60:63], v21\n" \
  "s_cmp_ge_u32 s45, 2\n" \
  "s_cbranch_scc1 .Lfz_A_d0\n" \
  "ds_read_b64_tr_b16 v[160:161], v241 offset:0\n" \
  "ds_read_b64_tr_b16 v[162:163], v241 offset:1024\n" \
  "ds_read_b64_tr_b16 v[164:165], v241 offset:4096\n" \
  "ds_read_b64_tr_b16 v[166:167], v241 offset:5120\n" \
  "ds_read_b64_tr_b16 v[168:169], v241 offset:8192\n" \
  "ds_read_b64_tr_b16 v[170:171], v241 offset:9216\n" \
  "ds_read_b64_tr_b16 v[172:173], v241 offset:12288\n" \
  "ds_read_b64_tr_b16 v[174:175], v241 offset:13312\n" \
  "v_add_u32 v241, 53248, v241\n" \
  ".Lfz_A_d0:\n" \
  "s_add_u32 m0, s96, 106496\n" \
  "s_nop 0\n" \
  "buffer_load_dwordx4 v224, s[4:7], s16 offen lds\n" \
  "s_add_u32 m0, s96, 122880\n" \
  "s_nop 0\n" \
  "buffer_load_dwordx4 v228, s[24:27], s17 offen lds\n" \
  "s_add_u32 m0, s96, 139264\n" \
  "s_nop 0\n" \
  "buffer_load_dwordx4 v228, s[28:31], s17 offen lds\n" \
  "s_waitcnt lgkmcnt(0)\n" \
  "v_mfma_f32_32x32x16_bf16 v[144:159], v[32:35], v[36:39], 0\n" \
  "s_add_u32 m0, s60, 106496\n" \
  "s_nop 0\n" \
  "buffer_load_dwordx4 v240, s[8:11], s18 offen lds\n" \
  "v_mfma_f32_32x32x16_bf16 v[144:159], v[40:43], v[44:47], v[144:159]\n" \
  "s_add_u32 m0, s97, 106496\n" \
  "s_nop 0\n" \
  "buffer_load_dwordx4 v225, s[4:7], s16 offen lds\n" \
  "s_add_u32 m0, s97, 122880\n" \
  "s_nop 0\n" \
  "buffer_load_dwordx4 v229, s[24:27], s17 offen lds\n" \
  "s_add_u32 m0, s97, 139264\n" \
  "s_nop 0\n" \
  "buffer_load_dwordx4 v229, s[28:31], s17 offen lds\n" \
  "v_mfma_f32_32x32x16_bf16 v[144:159], v[48:51], v[52:55], v[144:159]\n" \
  "v_mfma_f32_32x32x16_bf16 v[144:159], v[56:59], v[60:63], v[144:159]\n" \
    \
  "s_setprio 3\n" \
  "s_cmp_eq_u32 s45, 2\n" \
  "s_cbranch_scc1 .Lfz_A_e0\n" \
  "s_cmp_eq_u32 s45, 3\n" \
  "s_cbranch_scc1 .Lfz_A_c0\n" \
  "v_mfma_f32_32x32x16_bf16 v[128:143], v[196:199], v[160:163], 0\n" \
  "v_mfma_f32_32x32x16_bf16 v[128:143], v[196:199], v[164:167], v[128:143]\n" \
  "v_mfma_f32_32x32x16_bf16 v[128:143], v[196:199], v[168:171], v[128:143]\n" \
  "v_mfma_f32_32x32x16_bf16 v[128:143], v[196:199], v[172:175], v[128:143]\n" \
  "s_branch .Lfz_A_c0\n" \
  ".Lfz_A_e0:\n" \
  "v_mfma_f32_32x32x16_bf16 v[128:143], v[196:199], v[32:35], 0\n" \
  "v_mfma_f32_32x32x16_bf16 v[128:143], v[196:199], v[40:43], v[128:143]\n" \
  "v_mfma_f32_32x32x16_bf16 v[128:143], v[196:199], v[48:51], v[128:143]\n" \
  "v_mfma_f32_32x32x16_bf16 v[128:143], v[196:199], v[56:59], v[128:143]\n" \
  ".Lfz_A_c0:\n" \
  "v_add_u32 v17, 53248, v17\n" \
  "v_add_u32 v18, 53248, v18\n" \
  "v_add_u32 v19, 53248, v19\n" \
  "v_add_u32 v20, 53248, v20\n" \
  "v_add_u32 v21, 53248, v21\n" \
  "s_add_u32 s94, s94, 1\n" \
  "s_cmp_eq_u32 s94, 7\n" \
  "s_cselect_b32 s94, 0, s94\n" \
  "s_lshl_b32 s84, s94, 7\n" \
  "s_add_u32 s84, s84, s55\n" \
  "s_mul_i32 s85, s94, 229376\n" \
  "s_add_u32 s85, s85, s56\n" \
  "s_mul_i32 s86, s94, 57344\n" \
  "s_add_u32 s86, s86, s57\n" \
  "s_mov_b32 s32, 0x0fffffff\n" \
  "s_mov_b32 s33, 0x0fffffff\n" \
  "s_lshl2_add_u32 s35, s46, s45\n" \
  "s_lshl_b32 s35, s35, 8\n" \
  "s_add_u32 s35, s35, 9699328\n" \
  "s_lshl2_add_u32 s34, s46, s45\n" \
  "s_mul_i32 s34, s34, 144\n" \
  "s_lshl1_add_u32 s34, s49, s34\n" \
  "s_add_u32 s34, s34, 112\n" \
  "s_lshl_b32 s34, s34, 10\n" \
  "s_waitcnt vmcnt(8)\n" \
  "s_barrier\n" \
    \
  "s_setprio 0\n" \
  "s_add_u32 m0, s98, 106496\n" \
  "s_nop 0\n" \
  "buffer_load_dwordx4 v226, s[4:7], s16 offen lds\n" \
  "s_add_u32 m0, s98, 122880\n" \
  "s_nop 0\n" \
  "buffer_load_dwordx4 v230, s[24:27], s17 offen lds\n" \
  "s_add_u32 m0, s98, 139264\n" \
  "s_nop 0\n" \
  "buffer_load_dwordx4 v230, s[28:31], s17 offen lds\n" \
  "ds_read_b64_tr_b16 v[32:33], v17 offset:0\n" \
  "ds_read_b64_tr_b16 v[34:35], v17 offset:256\n" \
  "ds_read_b128 v[36:39], v18\n" \
  "ds_read_b64_tr_b16 v[40:41], v17 offset:1024\n" \
  "ds_read_b64_tr_b16 v[42:43], v17 offset:1280\n" \
  "ds_read_b128 v[44:47], v19\n" \
  "ds_read_b64_tr_b16 v[48:49], v17 offset:2048\n" \
  "ds_read_b64_tr_b16 v[50:51], v17 offset:2304\n" \
  "ds_read_b128 v[52:55], v20\n" \
  "ds_read_b64_tr_b16 v[56:57], v17 offset:3072\n" \
  "ds_read_b64_tr_b16 v[58:59], v17 offset:3328\n" \
  "ds_read_b128 v[60:63], v21\n" \
  "s_cmp_ge_u32 s45, 2\n" \
  "s_cbranch_scc1 .Lfz_A_d1\n" \
  "ds_read_b64_tr_b16 v[160:161], v241 offset:0\n" \
  "ds_read_b64_tr_b16 v[162:163], v241 offset:1024\n" \
  "ds_read_b64_tr_b16 v[164:165], v241 offset:4096\n" \
  "ds_read_b64_tr_b16 v[166:167], v241 offset:5120\n" \
  "ds_read_b64_tr_b16 v[168:169], v241 offset:8192\n" \
  "ds_read_b64_tr_b16 v[170:171], v241 offset:9216\n" \
  "ds_read_b64_tr_b16 v[172:173], v241 offset:12288\n" \
  "ds_read_b64_tr_b16 v[174:175], v241 offset:13312\n" \
  "v_add_u32 v241, 53248, v241\n" \
  ".Lfz_A_d1:\n" \
  "s_add_u32 m0, s99, 106496\n" \
  "s_nop 0\n" \
  "buffer_load_dwordx4 v227, s[4:7], s16 offen lds\n" \
  "s_add_u32 m0, s99, 122880\n" \
  "s_nop 0\n" \
  "buffer_load_dwordx4 v231, s[24:27], s17 offen lds\n" \
  "s_add_u32 m0, s99, 139264\n" \
  "s_nop 0\n" \
  "buffer_load_dwordx4 v231, s[28:31], s17 offen lds\n" \
  "s_waitcnt lgkmcnt(0)\n" \
  "s_setprio 3\n" \
  "v_mfma_f32_32x32x16_bf16 v[144:159], v[32:35], v[36:39], v[144:159]\n" \
  "s_setprio 0\n" \
  "s_add_u32 m0, s96, 0\n" \
  "s_nop 0\n" \
  "buffer_load_dwordx4 v224, s[4:7], s84 offen lds\n" \
  "s_add_u32 m0, s96, 16384\n" \
  "s_nop 0\n" \
  "buffer_load_dwordx4 v228, s[24:27], s85 offen lds\n" \
  "s_add_u32 m0, s96, 32768\n" \
  "s_nop 0\n" \
  "buffer_load_dwordx4 v228, s[28:31], s85 offen lds\n" \
  "s_setprio 3\n" \
  "v_mfma_f32_32x32x16_bf16 v[144:159], v[40:43], v[44:47], v[144:159]\n" \
  "s_setprio 0\n" \
  "s_add_u32 m0, s60, 0\n" \
  "s_nop 0\n" \
  "buffer_load_dwordx4 v240, s[8:11], s86 offen lds\n" \
  "s_setprio 3\n" \
  "v_mfma_f32_32x32x16_bf16 v[144:159], v[48:51], v[52:55], v[144:159]\n" \
  "s_setprio 0\n" \
  "s_add_u32 m0, s97, 0\n" \
  "s_nop 0\n" \
  "buffer_load_dwordx4 v225, s[4:7], s84 offen lds\n" \
  "s_add_u32 m0, s97, 16384\n" \
  "s_nop 0\n" \
  "buffer_load_dwordx4 v229, s[24:27], s85 offen lds\n" \
  "s_add_u32 m0, s97, 32768\n" \
  "s_nop 0\n" \
  "buffer_load_dwordx4 v229, s[28:31], s85 offen lds\n" \
  "s_setprio 3\n" \
  "v_mfma_f32_32x32x16_bf16 v[144:159], v[56:59], v[60:63], v[144:159]\n" \
  "s_setprio 0\n" \
    \
  "s_setprio 3\n" \
  "s_cmp_ge_u32 s46, 12\n" \
  "s_cbranch_scc1 .Lfz_A_c1\n" \
  "s_cmp_eq_u32 s45, 2\n" \
  "s_cbranch_scc1 .Lfz_A_e1\n" \
  "s_cmp_eq_u32 s45, 3\n" \
  "s_cbranch_scc1 .Lfz_A_c1\n" \
  "v_mfma_f32_32x32x16_bf16 v[128:143], v[196:199], v[160:163], v[128:143]\n" \
  "v_mfma_f32_32x32x16_bf16 v[128:143], v[196:199], v[164:167], v[128:143]\n" \
  "v_mfma_f32_32x32x16_bf16 v[128:143], v[196:199], v[168:171], v[128:143]\n" \
  "v_mfma_f32_32x32x16_bf16 v[128:143], v[196:199], v[172:175], v[128:143]\n" \
  "s_branch .Lfz_A_c1\n" \
  ".Lfz_A_e1:\n" \
  "v_mfma_f32_32x32x16_bf16 v[128:143], v[196:199], v[32:35], v[128:143]\n" \
  "v_mfma_f32_32x32x16_bf16 v[128:143], v[196:199], v[40:43], v[128:143]\n" \
  "v_mfma_f32_32x32x16_bf16 v[128:143], v[196:199], v[48:51], v[128:143]\n" \
  "v_mfma_f32_32x32x16_bf16 v[128:143], v[196:199], v[56:59], v[128:143]\n" \
  ".Lfz_A_c1:\n" \
  "v_add_u32 v17, 53248, v17\n" \
  "v_add_u32 v18, 53248, v18\n" \
  "v_add_u32 v19, 53248, v19\n" \
  "v_add_u32 v20, 53248, v20\n" \
  "v_add_u32 v21, 53248, v21\n" \
  "s_add_u32 s94, s94, 1\n" \
  "s_cmp_eq_u32 s94, 7\n" \
  "s_cselect_b32 s94, 0, s94\n" \
  "s_lshl_b32 s12, s94, 7\n" \
  "s_add_u32 s12, s12, s55\n" \
  "s_mul_i32 s13, s94, 229376\n" \
  "s_add_u32 s13, s13, s56\n" \
  "s_mul_i32 s14, s94, 57344\n" \
  "s_add_u32 s14, s14, s57\n" \
  "s_waitcnt vmcnt(20)\n" \
  "v_readfirstlane_b32 s51, v244\n" \
  "s_nop 0\n" \
  "s_add_u32 s51, s51, 1\n" \
  "v_mov_b32 v129, s51\n" \
  "buffer_store_dwordx2 v[128:129], v242, s[36:39], s93 offen sc1\n" \
  "s_waitcnt vmcnt(8)\n" \
  "s_barrier\n" \
    \
  "s_setprio 0\n" \
  "s_add_u32 m0, s98, 0\n" \
  "s_nop 0\n" \
  "buffer_load_dwordx4 v226, s[4:7], s84 offen lds\n" \
  "s_add_u32 m0, s98, 16384\n" \
  "s_nop 0\n" \
  "buffer_load_dwordx4 v230, s[24:27], s85 offen lds\n" \
  "s_add_u32 m0, s98, 32768\n" \
  "s_nop 0\n" \
  "buffer_load_dwordx4 v230, s[28:31], s85 offen lds\n" \
  "ds_read_b64_tr_b16 v[32:33], v17 offset:0\n" \
  "ds_read_b64_tr_b16 v[34:35], v17 offset:256\n" \
  "ds_read_b128 v[36:39], v18\n" \
  "ds_read_b64_tr_b16 v[40:41], v17 offset:1024\n" \
  "ds_read_b64_tr_b16 v[42:43], v17 offset:1280\n" \
  "ds_read_b128 v[44:47], v19\n" \
  "ds_read_b64_tr_b16 v[48:49], v17 offset:2048\n" \
  "ds_read_b64_tr_b16 v[50:51], v17 offset:2304\n" \
  "ds_read_b128 v[52:55], v20\n" \
  "ds_read_b64_tr_b16 v[56:57], v17 offset:3072\n" \
  "ds_read_b64_tr_b16 v[58:59], v17 offset:3328\n" \
  "ds_read_b128 v[60:63], v21\n" \
  "s_add_u32 m0, s99, 0\n" \
  "s_nop 0\n" \
  "buffer_load_dwordx4 v227, s[4:7], s84 offen lds\n" \
  "s_add_u32 m0, s99, 16384\n" \
  "s_nop 0\n" \
  "buffer_load_dwordx4 v231, s[24:27], s85 offen lds\n" \
  "s_add_u32 m0, s99, 32768\n" \
  "s_nop 0\n" \
  "buffer_load_dwordx4 v231, s[28:31], s85 offen lds\n" \
  "s_waitcnt lgkmcnt(0)\n" \
  "s_setprio 3\n" \
  "v_mfma_f32_32x32x16_bf16 v[144:159], v[32:35], v[36:39], v[144:159]\n" \
  "s_setprio 0\n" \
  "s_add_u32 m0, s96, 53248\n" \
  "s_nop 0\n" \
  "buffer_load_dwordx4 v224, s[4:7], s12 offen lds\n" \
  "s_add_u32 m0, s96, 69632\n" \
  "s_nop 0\n" \
  "buffer_load_dwordx4 v228, s[24:27], s13 offen lds\n" \
  "s_add_u32 m0, s96, 86016\n" \
  "s_nop 0\n" \
  "buffer_load_dwordx4 v228, s[28:31], s13 offen lds\n" \
  "s_setprio 3\n" \
  "v_mfma_f32_32x32x16_bf16 v[144:159], v[40:43], v[44:47], v[144:159]\n" \
  "s_setprio 0\n" \
  "s_add_u32 m0, s60, 53248\n" \
  "s_nop 0\n" \
  "buffer_load_dwordx4 v240, s[8:11], s14 offen lds\n" \
  "s_setprio 3\n" \
  "v_mfma_f32_32x32x16_bf16 v[144:159], v[48:51], v[52:55], v[144:159]\n" \
  "s_setprio 0\n" \
  "s_add_u32 m0, s97, 53248\n" \
  "s_nop 0\n" \
  "buffer_load_dwordx4 v225, s[4:7], s12 offen lds\n" \
  "s_add_u32 m0, s97, 69632\n" \
  "s_nop 0\n" \
  "buffer_load_dwordx4 v229, s[24:27], s13 offen lds\n" \
  "s_add_u32 m0, s97, 86016\n" \
  "s_nop 0\n" \
  "buffer_load_dwordx4 v229, s[28:31], s13 offen lds\n" \
  "s_setprio 3\n" \
  "v_mfma_f32_32x32x16_bf16 v[144:159], v[56:59], v[60:63], v[144:159]\n" \
  "s_setprio 0\n" \
  ".Lfz_A_c2:\n" \
  "v_add_u32 v17, 4294860800, v17\n" \
  "v_add_u32 v18, 4294860800, v18\n" \
  "v_add_u32 v19, 4294860800, v19\n" \
  "v_add_u32 v20, 4294860800, v20\n" \
  "v_add_u32 v21, 4294860800, v21\n" \
  "s_add_u32 s94, s94, 1\n" \
  "s_cmp_eq_u32 s94, 7\n" \
  "s_cselect_b32 s94, 0, s94\n" \
  "s_lshl_b32 s16, s94, 7\n" \
  "s_add_u32 s16, s16, s55\n" \
  "s_mul_i32 s17, s94, 229376\n" \
  "s_add_u32 s17, s17, s56\n" \
  "s_mul_i32 s18, s94, 57344\n" \
  "s_add_u32 s18, s18, s57\n" \
  "s_add_u32 s53, s45, 1\n" \
  "s_and_b32 s53, s53, 3\n" \
  "s_mul_i32 s54, s53, 12288\n" \
  "s_lshl_b32 s53, s53, 11\n" \
  "s_add_u32 s53, s53, 61440\n" \
  "v_add_u32 v26, s53, v2\n" \
  "s_add_u32 s54, s54, 0\n" \
  "v_add_u32 v12, s54, v2\n" \
  "s_add_u32 s53, s45, 2\n" \
  "s_and_b32 s53, s53, 3\n" \
  "s_mul_i32 s54, s53, 12288\n" \
  "s_lshl_b32 s53, s53, 11\n" \
  "s_add_u32 s53, s53, 61440\n" \
  "v_add_u32 v27, s53, v2\n" \
  "s_add_u32 s54, s54, 4096\n" \
  "v_add_u32 v13, s54, v2\n" \
  "s_add_u32 s53, s45, 3\n" \
  "s_and_b32 s53, s53, 3\n" \
  "s_mul_i32 s54, s53, 12288\n" \
  "s_lshl_b32 s53, s53, 11\n" \
  "s_add_u32 s53, s53, 61440\n" \
  "v_add_u32 v28, s53, v2\n" \
  "s_add_u32 s54, s54, 8192\n" \
  "v_add_u32 v14, s54, v2\n" \
  "s_add_u32 s53, s45, 4\n" \
  "s_and_b32 s53, s53, 3\n" \
  "s_mul_i32 s54, s53, 12288\n" \
  "s_lshl_b32 s53, s53, 11\n" \
  "s_add_u32 s53, s53, 61440\n" \
  "v_add_u32 v29, s53, v2\n" \
  "s_add_u32 s54, s54, 0\n" \
  "v_add_u32 v15, s54, v2\n" \
  "v_mov_b32 v8, 159744\n" \
  "v_mov_b32 v30, 0xdddddddd\n" \
  "s_lshl_b32 s53, s45, 10\n" \
  "s_add_u32 s53, s53, 53248\n" \
  "v_add_u32 v9, s53, v2\n" \
  "s_waitcnt vmcnt(7)\n" \
  "s_barrier\n" \
    \
  "s_setprio 0\n" \
  "s_add_u32 m0, s98, 53248\n" \
  "s_nop 0\n" \
  "buffer_load_dwordx4 v226, s[4:7], s12 offen lds\n" \
  "s_add_u32 m0, s98, 69632\n" \
  "s_nop 0\n" \
  "buffer_load_dwordx4 v230, s[24:27], s13 offen lds\n" \
  "s_add_u32 m0, s98, 86016\n" \
  "s_nop 0\n" \
  "buffer_load_dwordx4 v230, s[28:31], s13 offen lds\n" \
  "ds_read_b64_tr_b16 v[32:33], v17 offset:0\n" \
  "ds_read_b64_tr_b16 v[34:35], v17 offset:256\n" \
  "ds_read_b128 v[36:39], v18\n" \
  "ds_read_b64_tr_b16 v[40:41], v17 offset:1024\n" \
  "ds_read_b64_tr_b16 v[42:43], v17 offset:1280\n" \
  "ds_read_b128 v[44:47], v19\n" \
  "ds_read_b64_tr_b16 v[48:49], v17 offset:2048\n" \
  "ds_read_b64_tr_b16 v[50:51], v17 offset:2304\n" \
  "ds_read_b128 v[52:55], v20\n" \
  "ds_read_b64_tr_b16 v[56:57], v17 offset:3072\n" \
  "ds_read_b64_tr_b16 v[58:59], v17 offset:3328\n" \
  "ds_read_b128 v[60:63], v21\n" \
  "s_add_u32 m0, s99, 53248\n" \
  "s_nop 0\n" \
  "buffer_load_dwordx4 v227, s[4:7], s12 offen lds\n" \
  "s_add_u32 m0, s99, 69632\n" \
  "s_nop 0\n" \
  "buffer_load_dwordx4 v231, s[24:27], s13 offen lds\n" \
  "s_add_u32 m0, s99, 86016\n" \
  "s_nop 0\n" \
  "buffer_load_dwordx4 v231, s[28:31], s13 offen lds\n" \
  "s_waitcnt lgkmcnt(0)\n" \
  "s_setprio 3\n" \
  "v_mfma_f32_32x32x16_bf16 v[144:159], v[32:35], v[36:39], v[144:159]\n" \
  "s_setprio 0\n" \
  "s_add_u32 m0, s96, 106496\n" \
  "s_nop 0\n" \
  "buffer_load_dwordx4 v224, s[4:7], s16 offen lds\n" \
  "s_add_u32 m0, s96, 122880\n" \
  "s_nop 0\n" \
  "buffer_load_dwordx4 v228, s[24:27], s17 offen lds\n" \
  "s_add_u32 m0, s96, 139264\n" \
  "s_nop 0\n" \
  "buffer_load_dwordx4 v228, s[28:31], s17 offen lds\n" \
  "s_setprio 3\n" \
  "v_mfma_f32_32x32x16_bf16 v[144:159], v[40:43], v[44:47], v[144:159]\n" \
  "s_setprio 0\n" \
  "s_add_u32 m0, s60, 106496\n" \
  "s_nop 0\n" \
  "buffer_load_dwordx4 v240, s[8:11], s18 offen lds\n" \
  "s_setprio 3\n" \
  "v_mfma_f32_32x32x16_bf16 v[144:159], v[48:51], v[52:55], v[144:159]\n" \
  "s_setprio 0\n" \
  "s_add_u32 m0, s97, 106496\n" \
  "s_nop 0\n" \
  "buffer_load_dwordx4 v225, s[4:7], s16 offen lds\n" \
  "s_add_u32 m0, s97, 122880\n" \
  "s_nop 0\n" \
  "buffer_load_dwordx4 v229, s[24:27], s17 offen lds\n" \
  "s_add_u32 m0, s97, 139264\n" \
  "s_nop 0\n" \
  "buffer_load_dwordx4 v229, s[28:31], s17 offen lds\n" \
  "s_setprio 3\n" \
  "v_mfma_f32_32x32x16_bf16 v[144:159], v[56:59], v[60:63], v[144:159]\n" \
  "s_setprio 0\n" \
  ".Lfz_A_c3:\n" \
  "v_add_u32 v17, 53248, v17\n" \
  "v_add_u32 v18, 53248, v18\n" \
  "v_add_u32 v19, 53248, v19\n" \
  "v_add_u32 v20, 53248, v20\n" \
  "v_add_u32 v21, 53248, v21\n" \
  "s_add_u32 s94, s94, 1\n" \
  "s_cmp_eq_u32 s94, 7\n" \
  "s_cselect_b32 s94, 0, s94\n" \
  "s_lshl_b32 s84, s94, 7\n" \
  "s_add_u32 s84, s84, s55\n" \
  "s_mul_i32 s85, s94, 229376\n" \
  "s_add_u32 s85, s85, s56\n" \
  "s_mul_i32 s86, s94, 57344\n" \
  "s_add_u32 s86, s86, s57\n" \
  "v_and_b32 v11, 7, v0\n" \
  "s_lshl_b32 s53, s45, 10\n" \
  "s_add_u32 s53, s53, 9945088\n" \
  "v_lshl_add_u32 v11, v11, 7, s53\n" \
  "s_lshl_b32 s53, s45, 16\n" \
  "s_add_u32 s53, s53, 9437184\n" \
  "v_add_u32 v31, s53, v2\n" \
  "v_lshlrev_b32 v10, 3, v3\n" \
  "v_add_u32 v10, 768, v10\n" \
  "v_mul_u32_u24 v10, v4, v10\n" \
  "v_sub_u32 v10, v31, v10\n" \
  "v_lshlrev_b32 v16, 6, v4\n" \
  "v_sub_u32 v16, v31, v16\n" \
  "s_lshr_b32 s61, s2, 4\n" \
  "s_lshl_b32 s61, s61, 12\n" \
  "v_cmp_eq_u32 vcc, 28, v3\n" \
  "v_mov_b32 v248, 0x3f803f80\n" \
  "s_nop 0\n" \
  "v_cndmask_b32 v248, 0, v248, vcc\n" \
  "v_mov_b32 v249, v248\n" \
  "v_mov_b64 v[250:251], v[248:249]\n" \
  "v_mov_b64 v[252:253], v[248:249]\n" \
  "v_mov_b64 v[254:255], v[248:249]\n" \
  "v_mov_b64 v[200:201], v[248:249]\n" \
  "v_mov_b64 v[202:203], v[248:249]\n" \
  "v_mov_b64 v[204:205], v[248:249]\n" \
  "v_mov_b64 v[206:207], v[248:249]\n" \
  "v_mov_b64 v[216:217], v[248:249]\n" \
  "v_mov_b64 v[218:219], v[248:249]\n" \
  "v_mov_b64 v[220:221], v[248:249]\n" \
  "v_mov_b64 v[222:223], v[248:249]\n" \
  "v_mov_b64 v[232:233], v[248:249]\n" \
  "v_mov_b64 v[234:235], v[248:249]\n" \
  "v_mov_b64 v[236:237], v[248:249]\n" \
  "v_mov_b64 v[238:239], v[248:249]\n" \
  "s_waitcnt vmcnt(7)\n" \
  "s_barrier\n" \
    \
  "s_setprio 0\n" \
  "s_add_u32 m0, s98, 106496\n" \
  "s_nop 0\n" \
  "buffer_load_dwordx4 v226, s[4:7], s16 offen lds\n" \
  "s_add_u32 m0, s98, 122880\n" \
  "s_nop 0\n" \
  "buffer_load_dwordx4 v230, s[24:27], s17 offen lds\n" \
  "s_add_u32 m0, s98, 139264\n" \
  "s_nop 0\n" \
  "buffer_load_dwordx4 v230, s[28:31], s17 offen lds\n" \
  "ds_read_b64_tr_b16 v[32:33], v17 offset:0\n" \
  "ds_read_b64_tr_b16 v[34:35], v17 offset:256\n" \
  "ds_read_b128 v[36:39], v18\n" \
  "ds_read_b64_tr_b16 v[40:41], v17 offset:1024\n" \
  "ds_read_b64_tr_b16 v[42:43], v17 offset:1280\n" \
  "ds_read_b128 v[44:47], v19\n" \
  "ds_read_b64_tr_b16 v[48:49], v17 offset:2048\n" \
  "ds_read_b64_tr_b16 v[50:51], v17 offset:2304\n" \
  "ds_read_b128 v[52:55], v20\n" \
  "ds_read_b64_tr_b16 v[56:57], v17 offset:3072\n" \
  "ds_read_b64_tr_b16 v[58:59], v17 offset:3328\n" \
  "ds_read_b128 v[60:63], v21\n" \
  "s_add_u32 m0, s99, 106496\n" \
  "s_nop 0\n" \
  "buffer_load_dwordx4 v227, s[4:7], s16 offen lds\n" \
  "s_add_u32 m0, s99, 122880\n" \
  "s_nop 0\n" \
  "buffer_load_dwordx4 v231, s[24:27], s17 offen lds\n" \
  "s_add_u32 m0, s99, 139264\n" \
  "s_nop 0\n" \
  "buffer_load_dwordx4 v231, s[28:31], s17 offen lds\n" \
  "s_waitcnt lgkmcnt(0)\n" \
  "s_setprio 3\n" \
  "v_mfma_f32_32x32x16_bf16 v[144:159], v[32:35], v[36:39], v[144:159]\n" \
  "s_setprio 0\n" \
  "s_add_u32 m0, s96, 0\n" \
  "s_nop 0\n" \
  "buffer_load_dwordx4 v224, s[4:7], s84 offen lds\n" \
  "s_add_u32 m0, s96, 16384\n" \
  "s_nop 0\n" \
  "buffer_load_dwordx4 v228, s[24:27], s85 offen lds\n" \
  "s_add_u32 m0, s96, 32768\n" \
  "s_nop 0\n" \
  "buffer_load_dwordx4 v228, s[28:31], s85 offen lds\n" \
  "s_setprio 3\n" \
  "v_mfma_f32_32x32x16_bf16 v[144:159], v[40:43], v[44:47], v[144:159]\n" \
  "s_setprio 0\n" \
  "s_add_u32 m0, s60, 0\n" \
  "s_nop 0\n" \
  "buffer_load_dwordx4 v240, s[8:11], s86 offen lds\n" \
  "s_setprio 3\n" \
  "v_mfma_f32_32x32x16_bf16 v[144:159], v[48:51], v[52:55], v[144:159]\n" \
  "s_setprio 0\n" \
  "s_add_u32 m0, s97, 0\n" \
  "s_nop 0\n" \
  "buffer_load_dwordx4 v225, s[4:7], s84 offen lds\n" \
  "s_add_u32 m0, s97, 16384\n" \
  "s_nop 0\n" \
  "buffer_load_dwordx4 v229, s[24:27], s85 offen lds\n" \
  "s_add_u32 m0, s97, 32768\n" \
  "s_nop 0\n" \
  "buffer_load_dwordx4 v229, s[28:31], s85 offen lds\n" \
  "s_setprio 3\n" \
  "v_mfma_f32_32x32x16_bf16 v[144:159], v[56:59], v[60:63], v[144:159]\n" \
  "s_setprio 0\n" \
  ".Lfz_A_c4:\n" \
  "v_add_u32 v17, 53248, v17\n" \
  "v_add_u32 v18, 53248, v18\n" \
  "v_add_u32 v19, 53248, v19\n" \
  "v_add_u32 v20, 53248, v20\n" \
  "v_add_u32 v21, 53248, v21\n" \
  "s_waitcnt vmcnt(7)\n" \
  "s_barrier\n" \
    \
  "s_setprio 0\n" \
  "s_mov_b32 s54, 0x100000\n" \
  "s_add_u32 m0, s98, 0\n" \
  "s_nop 0\n" \
  "buffer_load_dwordx4 v226, s[4:7], s84 offen lds\n" \
  "s_add_u32 m0, s98, 16384\n" \
  "s_nop 0\n" \
  "buffer_load_dwordx4 v230, s[24:27], s85 offen lds\n" \
  "s_add_u32 m0, s98, 32768\n" \
  "s_nop 0\n" \
  "buffer_load_dwordx4 v230, s[28:31], s85 offen lds\n" \
  "ds_read_b64_tr_b16 v[32:33], v17 offset:0\n" \
  "ds_read_b64_tr_b16 v[34:35], v17 offset:256\n" \
  "ds_read_b128 v[36:39], v18\n" \
  "ds_read_b64_tr_b16 v[40:41], v17 offset:1024\n" \
  "ds_read_b64_tr_b16 v[42:43], v17 offset:1280\n" \
  "ds_read_b128 v[44:47], v19\n" \
  "ds_read_b64_tr_b16 v[48:49], v17 offset:2048\n" \
  "ds_read_b64_tr_b16 v[50:51], v17 offset:2304\n" \
  "ds_read_b128 v[52:55], v20\n" \
  "ds_read_b64_tr_b16 v[56:57], v17 offset:3072\n" \
  "ds_read_b64_tr_b16 v[58:59], v17 offset:3328\n" \
  "ds_read_b128 v[60:63], v21\n" \
  "s_add_u32 m0, s99, 0\n" \
  "s_nop 0\n" \
  "buffer_load_dwordx4 v227, s[4:7], s84 offen lds\n" \
  "s_add_u32 m0, s99, 16384\n" \
  "s_nop 0\n" \
  "buffer_load_dwordx4 v231, s[24:27], s85 offen lds\n" \
  "s_add_u32 m0, s99, 32768\n" \
  "s_nop 0\n" \
  "buffer_load_dwordx4 v231, s[28:31], s85 offen lds\n" \
  "s_waitcnt lgkmcnt(0)\n" \
  "s_setprio 3\n" \
  "v_mfma_f32_32x32x16_bf16 v[144:159], v[32:35], v[36:39], v[144:159]\n" \
  "s_setprio 0\n" \
  "buffer_load_dwordx2 v[128:129], v243, s[36:39], s92 offen offset:0 sc1\n" \
  "buffer_load_dwordx2 v[130:131], v242, s[36:39], s92 offen offset:512 sc1\n" \
  "buffer_load_dwordx2 v[132:133], v243, s[36:39], s92 offen offset:1024 sc1\n" \
  "buffer_load_dwordx2 v[134:135], v242, s[36:39], s92 offen offset:1536 sc1\n" \
  "buffer_load_dwordx2 v[136:137], v243, s[36:39], s92 offen offset:2048 sc1\n" \
  "buffer_load_dwordx2 v[138:139], v242, s[36:39], s92 offen offset:2560 sc1\n" \
  "buffer_load_dwordx2 v[140:141], v243, s[36:39], s92 offen offset:3072 sc1\n" \
  "buffer_load_dwordx2 v[142:143], v242, s[36:39], s92 offen offset:3584 sc1\n" \
  "s_setprio 3\n" \
  "v_mfma_f32_32x32x16_bf16 v[144:159], v[40:43], v[44:47], v[144:159]\n" \
  "s_setprio 0\n" \
  "buffer_load_dword v23, v11, s[36:39], 0 offen sc1\n" \
  "buffer_load_dwordx2 v[24:25], v242, s[36:39], s35 offen sc1\n" \
  "s_setprio 3\n" \
  "v_mfma_f32_32x32x16_bf16 v[144:159], v[48:51], v[52:55], v[144:159]\n" \
  "s_setprio 0\n" \
  "s_setprio 3\n" \
  "v_mfma_f32_32x32x16_bf16 v[144:159], v[56:59], v[60:63], v[144:159]\n" \
  "s_setprio 0\n" \
  "s_setprio 3\n" \
  "v_add_u32 v17, 4294860800, v17\n" \
  "v_add_u32 v18, 4294860800, v18\n" \
  "v_add_u32 v19, 4294860800, v19\n" \
  "v_add_u32 v20, 4294860800, v20\n" \
  "v_add_u32 v21, 4294860800, v21\n" \
  "s_waitcnt vmcnt(10)\n" \
  "s_barrier\n" \
    \
  "s_setprio 0\n" \
  "ds_read_b64_tr_b16 v[32:33], v17 offset:0\n" \
  "ds_read_b64_tr_b16 v[34:35], v17 offset:256\n" \
  "ds_read_b128 v[36:39], v18\n" \
  "ds_read_b64_tr_b16 v[40:41], v17 offset:1024\n" \
  "ds_read_b64_tr_b16 v[42:43], v17 offset:1280\n" \
  "ds_read_b128 v[44:47], v19\n" \
  "ds_read_b64_tr_b16 v[48:49], v17 offset:2048\n" \
  "ds_read_b64_tr_b16 v[50:51], v17 offset:2304\n" \
  "ds_read_b128 v[52:55], v20\n" \
  "ds_read_b64_tr_b16 v[56:57], v17 offset:3072\n" \
  "ds_read_b64_tr_b16 v[58:59], v17 offset:3328\n" \
  "ds_read_b128 v[60:63], v21\n" \
  "s_setprio 3\n" \
  "s_waitcnt vmcnt(0)\n" \
  ".Lfz_h1:\n" \
  "v_cmp_ne_u32 vcc, s51, v23\n" \
  "v_cmp_ne_u32 s[12:13], s51, v25\n" \
  "s_nop 0\n" \
  "s_or_b64 vcc, vcc, s[12:13]\n" \
  "s_cbranch_scc1 .Lfz_h1r\n" \
  ".Lfz_h1g:\n" \
  "s_mov_b64 exec, s[32:33]\n" \
  "buffer_load_dwordx4 v[200:203], v16, s[36:39], s61 offen offset:1792 sc1\n" \
  "buffer_load_dwordx4 v[204:207], v16, s[36:39], s61 offen offset:2688 sc1\n" \
  "s_mov_b64 exec, -1\n" \
  "buffer_load_dwordx4 v[192:195], v31, s[36:39], s61 offen sc1\n" \
  "buffer_load_dwordx4 v[196:199], v10, s[36:39], s61 offen offset:1280 sc1\n" \
  "s_add_u32 s61, s61, 4096\n" \
  "s_and_b32 s61, s61, 0xffff\n" \
  ".Lfz_cs:\n" \
  "v_max_u32 v245, v129, v131\n" \
  "v_min_u32 v246, v129, v131\n" \
  "v_max3_u32 v245, v133, v135, v245\n" \
  "v_min3_u32 v246, v133, v135, v246\n" \
  "v_max3_u32 v245, v137, v139, v245\n" \
  "v_min3_u32 v246, v137, v139, v246\n" \
  "v_max3_u32 v245, v141, v143, v245\n" \
  "v_min3_u32 v246, v141, v143, v246\n" \
  "v_cmp_ne_u32 vcc, s51, v245\n" \
  "v_cmp_ne_u32 s[12:13], s51, v246\n" \
  "s_nop 0\n" \
  "s_or_b64 vcc, vcc, s[12:13]\n" \
  "s_cbranch_scc1 .Lfz_csr\n" \
  ".Lfz_csg:\n" \
  "v_add_f32 v128, v128, v132\n" \
  "v_add_f32 v130, v130, v134\n" \
  "v_add_f32 v128, v128, v136\n" \
  "v_add_f32 v130, v130, v138\n" \
  "v_add_f32 v128, v128, v140\n" \
  "v_add_f32 v130, v130, v142\n" \
  "v_and_b32 v166, 0xffff0000, v128\n" \
  "v_sub_f32 v128, v128, v166\n" \
  "v_and_b32 v167, 0xffff0000, v128\n" \
  "v_sub_f32 v128, v128, v167\n" \
  "v_perm_b32 v164, v167, v166, s62\n" \
  "v_lshrrev_b32 v165, 16, v128\n" \
  "v_mov_b64 v[166:167], 0\n" \
  "v_and_b32 v170, 0xffff0000, v130\n" \
  "v_sub_f32 v130, v130, v170\n" \
  "v_and_b32 v171, 0xffff0000, v130\n" \
  "v_sub_f32 v130, v130, v171\n" \
  "v_perm_b32 v168, v171, v170, s62\n" \
  "v_lshrrev_b32 v169, 16, v130\n" \
  "v_mov_b64 v[170:171], 0\n" \
  "v_cmp_eq_u32 vcc, 0, v4\n" \
  "v_and_b32 v25, 0xffff0000, v24\n" \
  "v_and_b32 v160, 0xffff, v24\n" \
  "v_rcp_f32 v25, v25\n" \
  "v_lshl_or_b32 v160, v160, 16, v160\n" \
  "v_cndmask_b32 v160, 0, v160, vcc\n" \
  "v_mov_b32 v161, v160\n" \
  "v_mov_b64 v[162:163], 0\n" \
  "v_mul_f32 v24, 0x3e8b97f6, v25\n" \
  "s_waitcnt lgkmcnt(0)\n" \
  "v_mfma_f32_32x32x16_bf16 v[144:159], v[32:35], v[36:39], v[144:159]\n" \
  "v_mfma_f32_32x32x16_bf16 v[144:159], v[168:171], v[160:163], v[144:159]\n" \
  "v_mfma_f32_32x32x16_bf16 v[144:159], v[40:43], v[44:47], v[144:159]\n" \
  "ds_write_b64 v9, v[164:165]\n" \
  "ds_write_b64 v9, v[160:161] offset:4096\n" \
  "v_mfma_f32_32x32x16_bf16 v[144:159], v[48:51], v[52:55], v[144:159]\n" \
  "v_mfma_f32_32x32x16_bf16 v[144:159], v[56:59], v[60:63], v[144:159]\n" \
  ".Lfz_A_q:\n" \
  "s_mov_b64 exec, s[32:33]\n" \
  "buffer_load_dwordx4 v[216:219], v16, s[36:39], s61 offen offset:1792 sc1\n" \
  "buffer_load_dwordx4 v[220:223], v16, s[36:39], s61 offen offset:2688 sc1\n" \
  "s_mov_b64 exec, -1\n" \
  "buffer_load_dwordx4 v[208:211], v31, s[36:39], s61 offen sc1\n" \
  "buffer_load_dwordx4 v[212:215], v10, s[36:39], s61 offen offset:1280 sc1\n" \
  "s_add_u32 s61, s61, 4096\n" \
  "s_and_b32 s61, s61, 0xffff\n" \
  "s_nop 3\n" \
  "v_mul_f32 v144, v24, v144\n" \
  "v_mul_f32 v145, v24, v145\n" \
  "v_mul_f32 v146, v24, v146\n" \
  "v_mul_f32 v147, v24, v147\n" \
  "v_mul_f32 v148, v24, v148\n" \
  "v_mul_f32 v149, v24, v149\n" \
  "v_mul_f32 v150, v24, v150\n" \
  "v_mul_f32 v151, v24, v151\n" \
  "v_mul_f32 v152, v24, v152\n" \
  "v_mul_f32 v153, v24, v153\n" \
  "v_mul_f32 v154, v24, v154\n" \
  "v_mul_f32 v155, v24, v155\n" \
  "v_mul_f32 v156, v24, v156\n" \
  "v_mul_f32 v157, v24, v157\n" \
  "v_mul_f32 v158, v24, v158\n" \
  "v_mul_f32 v159, v24, v159\n" \
  "v_cvt_pk_bf16_f32 v56, v144, v145\n" \
  "v_cvt_pk_bf16_f32 v57, v146, v147\n" \
  "v_cvt_pk_bf16_f32 v58, v148, v149\n" \
  "v_cvt_pk_bf16_f32 v59, v150, v151\n" \
  "v_cvt_pk_bf16_f32 v60, v152, v153\n" \
  "v_cvt_pk_bf16_f32 v61, v154, v155\n" \
  "v_cvt_pk_bf16_f32 v62, v156, v157\n" \
  "v_cvt_pk_bf16_f32 v63, v158, v159\n" \
  "v_cndmask_b32 v62, 0, v62, vcc\n" \
  "v_cndmask_b32 v63, 0, v63, vcc\n" \
  "ds_write_b128 v29, v[56:59]\n" \
  "ds_write_b128 v29, v[60:63] offset:1024\n" \
  "s_waitcnt lgkmcnt(0)\n" \
  "s_barrier\n" \
  "s_setprio 1\n" \
  "ds_read_b128 v[32:35], v26\n" \
  "ds_read_b128 v[36:39], v26 offset:1024\n" \
  "ds_read_b128 v[40:43], v27\n" \
  "ds_read_b128 v[44:47], v27 offset:1024\n" \
  "ds_read_b128 v[48:51], v28\n" \
  "ds_read_b128 v[52:55], v28 offset:1024\n" \
  "s_mov_b64 exec, s[32:33]\n" \
  "buffer_load_dwordx4 v[232:235], v16, s[36:39], s61 offen offset:1792 sc1\n" \
  "buffer_load_dwordx4 v[236:239], v16, s[36:39], s61 offen offset:2688 sc1\n" \
  "s_mov_b64 exec, -1\n" \
  "buffer_load_dwordx4 v[224:227], v31, s[36:39], s61 offen sc1\n" \
  "buffer_load_dwordx4 v[228:231], v10, s[36:39], s61 offen offset:1280 sc1\n" \
  "s_add_u32 s61, s61, 4096\n" \
  "s_and_b32 s61, s61, 0xffff\n" \
    \
  "s_mov_b32 s60, 4\n" \
  "s_waitcnt vmcnt(8) lgkmcnt(0)\n" \
  "v_mfma_f32_32x32x16_bf16 v[128:143], v[192:195], v[32:35], 0\n" \
  "v_mfma_f32_32x32x16_bf16 v[128:143], v[196:199], v[36:39], v[128:143]\n" \
  "v_mfma_f32_32x32x16_bf16 v[144:159], v[192:195], v[40:43], 0\n" \
  "s_nop 10\n" \
  "v_exp_f32 v128, v128\n" \
  "v_exp_f32 v129, v129\n" \
  "v_exp_f32 v130, v130\n" \
  "v_mfma_f32_32x32x16_bf16 v[144:159], v[196:199], v[44:47], v[144:159]\n" \
  "v_exp_f32 v131, v131\n" \
  "v_exp_f32 v132, v132\n" \
  "v_exp_f32 v133, v133\n" \
  "v_exp_f32 v134, v134\n" \
  "v_exp_f32 v135, v135\n" \
  "v_exp_f32 v136, v136\n" \
  "v_exp_f32 v137, v137\n" \
  "v_exp_f32 v138, v138\n" \
  "v_exp_f32 v139, v139\n" \
  "v_exp_f32 v140, v140\n" \
  "v_exp_f32 v141, v141\n" \
  "v_exp_f32 v142, v142\n" \
  "v_exp_f32 v143, v143\n" \
  "v_mfma_f32_32x32x16_bf16 v[160:175], v[192:195], v[48:51], 0\n" \
  "v_exp_f32 v144, v144\n" \
  "v_exp_f32 v145, v145\n" \
  "v_exp_f32 v146, v146\n" \
  "v_mfma_f32_32x32x16_bf16 v[160:175], v[196:199], v[52:55], v[160:175]\n" \
  "v_exp_f32 v147, v147\n" \
  "v_exp_f32 v148, v148\n" \
  "v_exp_f32 v149, v149\n" \
  "v_smfmac_f32_32x32x32_bf16 v[64:79], v[200:203], v[128:135], v30\n" \
  "s_mov_b64 exec, s[32:33]\n" \
  "buffer_load_dwordx4 v[248:251], v16, s[36:39], s61 offen offset:1792 sc1\n" \
  "buffer_load_dwordx4 v[252:255], v16, s[36:39], s61 offen offset:2688 sc1\n" \
  "s_mov_b64 exec, -1\n" \
  "v_exp_f32 v150, v150\n" \
  "v_exp_f32 v151, v151\n" \
  "v_exp_f32 v152, v152\n" \
  "v_smfmac_f32_32x32x32_bf16 v[64:79], v[204:207], v[136:143], v30\n" \
  "buffer_load_dwordx4 v[240:243], v31, s[36:39], s61 offen sc1\n" \
  "buffer_load_dwordx4 v[244:247], v10, s[36:39], s61 offen offset:1280 sc1\n" \
  "v_exp_f32 v153, v153\n" \
  "v_exp_f32 v154, v154\n" \
  "v_exp_f32 v155, v155\n" \
  "v_exp_f32 v156, v156\n" \
  "v_exp_f32 v157, v157\n" \
  "v_exp_f32 v158, v158\n" \
  "v_exp_f32 v159, v159\n" \
  "v_mfma_f32_32x32x16_bf16 v[176:191], v[192:195], v[56:59], 0\n" \
  "s_add_u32 s61, s61, 4096\n" \
  "v_exp_f32 v160, v160\n" \
  "v_exp_f32 v161, v161\n" \
  "v_exp_f32 v162, v162\n" \
  "v_mfma_f32_32x32x16_bf16 v[176:191], v[196:199], v[60:63], v[176:191]\n" \
  "s_and_b32 s61, s61, 0xffff\n" \
  "v_exp_f32 v163, v163\n" \
  "v_exp_f32 v164, v164\n" \
  "v_exp_f32 v165, v165\n" \
  "v_smfmac_f32_32x32x32_bf16 v[80:95], v[200:203], v[144:151], v30\n" \
  "v_exp_f32 v166, v166\n" \
  "v_exp_f32 v167, v167\n" \
  "v_exp_f32 v168, v168\n" \
  "v_smfmac_f32_32x32x32_bf16 v[80:95], v[204:207], v[152:159], v30\n" \
  "s_waitcnt vmcnt(8)\n" \
  "v_exp_f32 v169, v169\n" \
  "v_exp_f32 v170, v170\n" \
  "v_exp_f32 v171, v171\n" \
  "v_exp_f32 v172, v172\n" \
  "v_exp_f32 v173, v173\n" \
  "v_exp_f32 v174, v174\n" \
  "v_exp_f32 v175, v175\n" \
  "v_mfma_f32_32x32x16_bf16 v[128:143], v[208:211], v[32:35], 0\n" \
  "v_exp_f32 v176, v176\n" \
  "v_exp_f32 v177, v177\n" \
  "v_exp_f32 v178, v178\n" \
  "v_mfma_f32_32x32x16_bf16 v[128:143], v[212:215], v[36:39], v[128:143]\n" \
  "v_exp_f32 v179, v179\n" \
  "v_exp_f32 v180, v180\n" \
  "v_exp_f32 v181, v181\n" \
  "v_smfmac_f32_32x32x32_bf16 v[96:111], v[200:203], v[160:167], v30\n" \
  "v_exp_f32 v182, v182\n" \
  "v_exp_f32 v183, v183\n" \
  "v_exp_f32 v184, v184\n" \
  "v_smfmac_f32_32x32x32_bf16 v[96:111], v[204:207], v[168:175], v30\n" \
  "v_exp_f32 v185, v185\n" \
  "v_exp_f32 v186, v186\n" \
  "v_exp_f32 v187, v187\n" \
  "v_exp_f32 v188, v188\n" \
  "v_exp_f32 v189, v189\n" \
  "v_exp_f32 v190, v190\n" \
  "v_exp_f32 v191, v191\n" \
  "v_mfma_f32_32x32x16_bf16 v[144:159], v[208:211], v[40:43], 0\n" \
  "v_exp_f32 v128, v128\n" \
  "v_exp_f32 v129, v129\n" \
  "v_exp_f32 v130, v130\n" \
  "v_mfma_f32_32x32x16_bf16 v[144:159], v[212:215], v[44:47], v[144:159]\n" \
  "v_exp_f32 v131, v131\n" \
  "v_exp_f32 v132, v132\n" \
  "v_exp_f32 v133, v133\n" \
  "v_smfmac_f32_32x32x32_bf16 v[112:127], v[200:203], v[176:183], v30\n" \
  "v_exp_f32 v134, v134\n" \
  "v_exp_f32 v135, v135\n" \
  "v_exp_f32 v136, v136\n" \
  "v_smfmac_f32_32x32x32_bf16 v[112:127], v[204:207], v[184:191], v30\n" \
  "v_exp_f32 v137, v137\n" \
  "v_exp_f32 v138, v138\n" \
  "v_exp_f32 v139, v139\n" \
  "v_exp_f32 v140, v140\n" \
  "v_exp_f32 v141, v141\n" \
  "v_exp_f32 v142, v142\n" \
  "v_exp_f32 v143, v143\n" \
  "v_mfma_f32_32x32x16_bf16 v[160:175], v[208:211], v[48:51], 0\n" \
  "v_exp_f32 v144, v144\n" \
  "v_exp_f32 v145, v145\n" \
  "v_exp_f32 v146, v146\n" \
  "v_mfma_f32_32x32x16_bf16 v[160:175], v[212:215], v[52:55], v[160:175]\n" \
  "v_exp_f32 v147, v147\n" \
  "v_exp_f32 v148, v148\n" \
  "v_exp_f32 v149, v149\n" \
  "v_smfmac_f32_32x32x32_bf16 v[64:79], v[216:219], v[128:135], v30\n" \
  "s_mov_b64 exec, s[32:33]\n" \
  "buffer_load_dwordx4 v[200:203], v16, s[36:39], s61 offen offset:1792 sc1\n" \
  "buffer_load_dwordx4 v[204:207], v16, s[36:39], s61 offen offset:2688 sc1\n" \
  "s_mov_b64 exec, -1\n" \
  "v_exp_f32 v150, v150\n" \
  "v_exp_f32 v151, v151\n" \
  "v_exp_f32 v152, v152\n" \
  "v_smfmac_f32_32x32x32_bf16 v[64:79], v[220:223], v[136:143], v30\n" \
  "buffer_load_dwordx4 v[192:195], v31, s[36:39], s61 offen sc1\n" \
  "buffer_load_dwordx4 v[196:199], v10, s[36:39], s61 offen offset:1280 sc1\n" \
  "v_exp_f32 v153, v153\n" \
  "v_exp_f32 v154, v154\n" \
  "v_exp_f32 v155, v155\n" \
  "v_exp_f32 v156, v156\n" \
  "v_exp_f32 v157, v157\n" \
  "v_exp_f32 v158, v158\n" \
  "v_exp_f32 v159, v159\n" \
  "v_mfma_f32_32x32x16_bf16 v[176:191], v[208:211], v[56:59], 0\n" \
  "s_add_u32 s61, s61, 4096\n" \
  "v_exp_f32 v160, v160\n" \
  "v_exp_f32 v161, v161\n" \
  "v_exp_f32 v162, v162\n" \
  "v_mfma_f32_32x32x16_bf16 v[176:191], v[212:215], v[60:63], v[176:191]\n" \
  "s_and_b32 s61, s61, 0xffff\n" \
  "v_exp_f32 v163, v163\n" \
  "v_exp_f32 v164, v164\n" \
  "v_exp_f32 v165, v165\n" \
  "v_smfmac_f32_32x32x32_bf16 v[80:95], v[216:219], v[144:151], v30\n" \
  "v_exp_f32 v166, v166\n" \
  "v_exp_f32 v167, v167\n" \
  "v_exp_f32 v168, v168\n" \
  "v_smfmac_f32_32x32x32_bf16 v[80:95], v[220:223], v[152:159], v30\n" \
  "s_waitcnt vmcnt(8)\n" \
  "v_exp_f32 v169, v169\n" \
  "v_exp_f32 v170, v170\n" \
  "v_exp_f32 v171, v171\n" \
  "v_exp_f32 v172, v172\n" \
  "v_exp_f32 v173, v173\n" \
  "v_exp_f32 v174, v174\n" \
  "v_exp_f32 v175, v175\n" \
  "v_mfma_f32_32x32x16_bf16 v[128:143], v[224:227], v[32:35], 0\n" \
  "v_exp_f32 v176, v176\n" \
  "v_exp_f32 v177, v177\n" \
  "v_exp_f32 v178, v178\n" \
  "v_mfma_f32_32x32x16_bf16 v[128:143], v[228:231], v[36:39], v[128:143]\n" \
  "v_exp_f32 v179, v179\n" \
  "v_exp_f32 v180, v180\n" \
  "v_exp_f32 v181, v181\n" \
  "v_smfmac_f32_32x32x32_bf16 v[96:111], v[216:219], v[160:167], v30\n" \
  "v_exp_f32 v182, v182\n" \
  "v_exp_f32 v183, v183\n" \
  "v_exp_f32 v184, v184\n" \
  "v_smfmac_f32_32x32x32_bf16 v[96:111], v[220:223], v[168:175], v30\n" \
  "v_exp_f32 v185, v185\n" \
  "v_exp_f32 v186, v186\n" \
  "v_exp_f32 v187, v187\n" \
  "v_exp_f32 v188, v188\n" \
  "v_exp_f32 v189, v189\n" \
  "v_exp_f32 v190, v190\n" \
  "v_exp_f32 v191, v191\n" \
  "v_mfma_f32_32x32x16_bf16 v[144:159], v[224:227], v[40:43], 0\n" \
  "v_exp_f32 v128, v128\n" \
  "v_exp_f32 v129, v129\n" \
  "v_exp_f32 v130, v130\n" \
  "v_mfma_f32_32x32x16_bf16 v[144:159], v[228:231], v[44:47], v[144:159]\n" \
  "v_exp_f32 v131, v131\n" \
  "v_exp_f32 v132, v132\n" \
  "v_exp_f32 v133, v133\n" \
  "v_smfmac_f32_32x32x32_bf16 v[112:127], v[216:219], v[176:183], v30\n" \
  "v_exp_f32 v134, v134\n" \
  "v_exp_f32 v135, v135\n" \
  "v_exp_f32 v136, v136\n" \
  "v_smfmac_f32_32x32x32_bf16 v[112:127], v[220:223], v[184:191], v30\n" \
  "v_exp_f32 v137, v137\n" \
  "v_exp_f32 v138, v138\n" \
  "v_exp_f32 v139, v139\n" \
  "v_exp_f32 v140, v140\n" \
  "v_exp_f32 v141, v141\n" \
  "v_exp_f32 v142, v142\n" \
  "v_exp_f32 v143, v143\n" \
  "v_mfma_f32_32x32x16_bf16 v[160:175], v[224:227], v[48:51], 0\n" \
  "v_exp_f32 v144, v144\n" \
  "v_exp_f32 v145, v145\n" \
  "v_exp_f32 v146, v146\n" \
  "v_mfma_f32_32x32x16_bf16 v[160:175], v[228:231], v[52:55], v[160:175]\n" \
  "v_exp_f32 v147, v147\n" \
  "v_exp_f32 v148, v148\n" \
  "v_exp_f32 v149, v149\n" \
  "v_smfmac_f32_32x32x32_bf16 v[64:79], v[232:235], v[128:135], v30\n" \
  "s_mov_b64 exec, s[32:33]\n" \
  "buffer_load_dwordx4 v[216:219], v16, s[36:39], s61 offen offset:1792 sc1\n" \
  "buffer_load_dwordx4 v[220:223], v16, s[36:39], s61 offen offset:2688 sc1\n" \
  "s_mov_b64 exec, -1\n" \
  "v_exp_f32 v150, v150\n" \
  "v_exp_f32 v151, v151\n" \
  "v_exp_f32 v152, v152\n" \
  "v_smfmac_f32_32x32x32_bf16 v[64:79], v[236:239], v[136:143], v30\n" \
  "buffer_load_dwordx4 v[208:211], v31, s[36:39], s61 offen sc1\n" \
  "buffer_load_dwordx4 v[212:215], v10, s[36:39], s61 offen offset:1280 sc1\n" \
  "v_exp_f32 v153, v153\n" \
  "v_exp_f32 v154, v154\n" \
  "v_exp_f32 v155, v155\n" \
  "v_exp_f32 v156, v156\n" \
  "v_exp_f32 v157, v157\n" \
  "v_exp_f32 v158, v158\n" \
  "v_exp_f32 v159, v159\n" \
  "v_mfma_f32_32x32x16_bf16 v[176:191], v[224:227], v[56:59], 0\n" \
  "s_add_u32 s61, s61, 4096\n" \
  "v_exp_f32 v160, v160\n" \
  "v_exp_f32 v161, v161\n" \
  "v_exp_f32 v162, v162\n" \
  "v_mfma_f32_32x32x16_bf16 v[176:191], v[228:231], v[60:63], v[176:191]\n" \
  "s_and_b32 s61, s61, 0xffff\n" \
  "v_exp_f32 v163, v163\n" \
  "v_exp_f32 v164, v164\n" \
  "v_exp_f32 v165, v165\n" \
  "v_smfmac_f32_32x32x32_bf16 v[80:95], v[232:235], v[144:151], v30\n" \
  "v_exp_f32 v166, v166\n" \
  "v_exp_f32 v167, v167\n" \
  "v_exp_f32 v168, v168\n" \
  "v_smfmac_f32_32x32x32_bf16 v[80:95], v[236:239], v[152:159], v30\n" \
  "s_waitcnt vmcnt(8)\n" \
  "v_exp_f32 v169, v169\n" \
  "v_exp_f32 v170, v170\n" \
  "v_exp_f32 v171, v171\n" \
  "v_exp_f32 v172, v172\n" \
  "v_exp_f32 v173, v173\n" \
  "v_exp_f32 v174, v174\n" \
  "v_exp_f32 v175, v175\n" \
  "v_mfma_f32_32x32x16_bf16 v[128:143], v[240:243], v[32:35], 0\n" \
  "v_exp_f32 v176, v176\n" \
  "v_exp_f32 v177, v177\n" \
  "v_exp_f32 v178, v178\n" \
  "v_mfma_f32_32x32x16_bf16 v[128:143], v[244:247], v[36:39], v[128:143]\n" \
  "v_exp_f32 v179, v179\n" \
  "v_exp_f32 v180, v180\n" \
  "v_exp_f32 v181, v181\n" \
  "v_smfmac_f32_32x32x32_bf16 v[96:111], v[232:235], v[160:167], v30\n" \
  "v_exp_f32 v182, v182\n" \
  "v_exp_f32 v183, v183\n" \
  "v_exp_f32 v184, v184\n" \
  "v_smfmac_f32_32x32x32_bf16 v[96:111], v[236:239], v[168:175], v30\n" \
  "v_exp_f32 v185, v185\n" \
  "v_exp_f32 v186, v186\n" \
  "v_exp_f32 v187, v187\n" \
  "v_exp_f32 v188, v188\n" \
  "v_exp_f32 v189, v189\n" \
  "v_exp_f32 v190, v190\n" \
  "v_exp_f32 v191, v191\n" \
  "v_mfma_f32_32x32x16_bf16 v[144:159], v[240:243], v[40:43], 0\n" \
  "v_exp_f32 v128, v128\n" \
  "v_exp_f32 v129, v129\n" \
  "v_exp_f32 v130, v130\n" \
  "v_mfma_f32_32x32x16_bf16 v[144:159], v[244:247], v[44:47], v[144:159]\n" \
  "v_exp_f32 v131, v131\n" \
  "v_exp_f32 v132, v132\n" \
  "v_exp_f32 v133, v133\n" \
  "v_smfmac_f32_32x32x32_bf16 v[112:127], v[232:235], v[176:183], v30\n" \
  "v_exp_f32 v134, v134\n" \
  "v_exp_f32 v135, v135\n" \
  "v_exp_f32 v136, v136\n" \
  "v_smfmac_f32_32x32x32_bf16 v[112:127], v[236:239], v[184:191], v30\n" \
  "v_exp_f32 v137, v137\n" \
  "v_exp_f32 v138, v138\n" \
  "v_exp_f32 v139, v139\n" \
  "v_exp_f32 v140, v140\n" \
  "v_exp_f32 v141, v141\n" \
  "v_exp_f32 v142, v142\n" \
  "v_exp_f32 v143, v143\n" \
  "v_mfma_f32_32x32x16_bf16 v[160:175], v[240:243], v[48:51], 0\n" \
  "v_exp_f32 v144, v144\n" \
  "v_exp_f32 v145, v145\n" \
  "v_exp_f32 v146, v146\n" \
  "v_mfma_f32_32x32x16_bf16 v[160:175], v[244:247], v[52:55], v[160:175]\n" \
  "v_exp_f32 v147, v147\n" \
  "v_exp_f32 v148, v148\n" \
  "v_exp_f32 v149, v149\n" \
  "v_smfmac_f32_32x32x32_bf16 v[64:79], v[248:251], v[128:135], v30\n" \
  "s_mov_b64 exec, s[32:33]\n" \
  "buffer_load_dwordx4 v[232:235], v16, s[36:39], s61 offen offset:1792 sc1\n" \
  "buffer_load_dwordx4 v[236:239], v16, s[36:39], s61 offen offset:2688 sc1\n" \
  "s_mov_b64 exec, -1\n" \
  "v_exp_f32 v150, v150\n" \
  "v_exp_f32 v151, v151\n" \
  "v_exp_f32 v152, v152\n" \
  "v_smfmac_f32_32x32x32_bf16 v[64:79], v[252:255], v[136:143], v30\n" \
  "buffer_load_dwordx4 v[224:227], v31, s[36:39], s61 offen sc1\n" \
  "buffer_load_dwordx4 v[228:231], v10, s[36:39], s61 offen offset:1280 sc1\n" \
  "v_exp_f32 v153, v153\n" \
  "v_exp_f32 v154, v154\n" \
  "v_exp_f32 v155, v155\n" \
  "v_exp_f32 v156, v156\n" \
  "v_exp_f32 v157, v157\n" \
  "v_exp_f32 v158, v158\n" \
  "v_exp_f32 v159, v159\n" \
  "v_mfma_f32_32x32x16_bf16 v[176:191], v[240:243], v[56:59], 0\n" \
  "s_add_u32 s61, s61, 4096\n" \
  "v_exp_f32 v160, v160\n" \
  "v_exp_f32 v161, v161\n" \
  "v_exp_f32 v162, v162\n" \
  "v_mfma_f32_32x32x16_bf16 v[176:191], v[244:247], v[60:63], v[176:191]\n" \
  "s_and_b32 s61, s61, 0xffff\n" \
  "v_exp_f32 v163, v163\n" \
  "v_exp_f32 v164, v164\n" \
  "v_exp_f32 v165, v165\n" \
  "v_smfmac_f32_32x32x32_bf16 v[80:95], v[248:251], v[144:151], v30\n" \
  "v_exp_f32 v166, v166\n" \
  "v_exp_f32 v167, v167\n" \
  "v_exp_f32 v168, v168\n" \
  "v_smfmac_f32_32x32x32_bf16 v[80:95], v[252:255], v[152:159], v30\n" \
  "s_waitcnt vmcnt(8)\n" \
  "v_exp_f32 v169, v169\n" \
  "v_exp_f32 v170, v170\n" \
  "v_exp_f32 v171, v171\n" \
  "v_exp_f32 v172, v172\n" \
  "v_exp_f32 v173, v173\n" \
  "v_exp_f32 v174, v174\n" \
  "v_exp_f32 v175, v175\n" \
  "v_mfma_f32_32x32x16_bf16 v[128:143], v[192:195], v[32:35], 0\n" \
  "v_exp_f32 v176, v176\n" \
  "v_exp_f32 v177, v177\n" \
  "v_exp_f32 v178, v178\n" \
  "v_mfma_f32_32x32x16_bf16 v[128:143], v[196:199], v[36:39], v[128:143]\n" \
  "v_exp_f32 v179, v179\n" \
  "v_exp_f32 v180, v180\n" \
  "v_exp_f32 v181, v181\n" \
  "v_smfmac_f32_32x32x32_bf16 v[96:111], v[248:251], v[160:167], v30\n" \
  "v_exp_f32 v182, v182\n" \
  "v_exp_f32 v183, v183\n" \
  "v_exp_f32 v184, v184\n" \
  "v_smfmac_f32_32x32x32_bf16 v[96:111], v[252:255], v[168:175], v30\n" \
  "v_exp_f32 v185, v185\n" \
  "v_exp_f32 v186, v186\n" \
  "v_exp_f32 v187, v187\n" \
  "v_exp_f32 v188, v188\n" \
  "v_exp_f32 v189, v189\n" \
  "v_exp_f32 v190, v190\n" \
  "v_exp_f32 v191, v191\n" \
  "v_mfma_f32_32x32x16_bf16 v[144:159], v[192:195], v[40:43], 0\n" \
  "v_exp_f32 v128, v128\n" \
  "v_exp_f32 v129, v129\n" \
  "v_exp_f32 v130, v130\n" \
  "v_mfma_f32_32x32x16_bf16 v[144:159], v[196:199], v[44:47], v[144:159]\n" \
  "v_exp_f32 v131, v131\n" \
  "v_exp_f32 v132, v132\n" \
  "v_exp_f32 v133, v133\n" \
  "v_smfmac_f32_32x32x32_bf16 v[112:127], v[248:251], v[176:183], v30\n" \
  "v_exp_f32 v134, v134\n" \
  "v_exp_f32 v135, v135\n" \
  "v_exp_f32 v136, v136\n" \
  "v_smfmac_f32_32x32x32_bf16 v[112:127], v[252:255], v[184:191], v30\n" \
  "v_exp_f32 v137, v137\n" \
  "v_exp_f32 v138, v138\n" \
  "v_exp_f32 v139, v139\n" \
  "v_exp_f32 v140, v140\n" \
  "v_exp_f32 v141, v141\n" \
  "v_exp_f32 v142, v142\n" \
  "v_exp_f32 v143, v143\n" \
  "v_mfma_f32_32x32x16_bf16 v[160:175], v[192:195], v[48:51], 0\n" \
  "v_exp_f32 v144, v144\n" \
  "v_exp_f32 v145, v145\n" \
  "v_exp_f32 v146, v146\n" \
  "v_mfma_f32_32x32x16_bf16 v[160:175], v[196:199], v[52:55], v[160:175]\n" \
  "v_exp_f32 v147, v147\n" \
  "v_exp_f32 v148, v148\n" \
  "v_exp_f32 v149, v149\n" \
  "v_smfmac_f32_32x32x32_bf16 v[64:79], v[200:203], v[128:135], v30\n" \
  "s_mov_b64 exec, s[32:33]\n" \
  "buffer_load_dwordx4 v[248:251], v16, s[36:39], s61 offen offset:1792 sc1\n" \
  "buffer_load_dwordx4 v[252:255], v16, s[36:39], s61 offen offset:2688 sc1\n" \
  "s_mov_b64 exec, -1\n" \
  "v_exp_f32 v150, v150\n" \
  "v_exp_f32 v151, v151\n" \
  "v_exp_f32 v152, v152\n" \
  "v_smfmac_f32_32x32x32_bf16 v[64:79], v[204:207], v[136:143], v30\n" \
  "buffer_load_dwordx4 v[240:243], v31, s[36:39], s61 offen sc1\n" \
  "buffer_load_dwordx4 v[244:247], v10, s[36:39], s61 offen offset:1280 sc1\n" \
  "v_exp_f32 v153, v153\n" \
  "v_exp_f32 v154, v154\n" \
  "v_exp_f32 v155, v155\n" \
  "v_exp_f32 v156, v156\n" \
  "v_exp_f32 v157, v157\n" \
  "v_exp_f32 v158, v158\n" \
  "v_exp_f32 v159, v159\n" \
  "v_mfma_f32_32x32x16_bf16 v[176:191], v[192:195], v[56:59], 0\n" \
  "s_add_u32 s61, s61, 4096\n" \
  "v_exp_f32 v160, v160\n" \
  "v_exp_f32 v161, v161\n" \
  "v_exp_f32 v162, v162\n" \
  "v_mfma_f32_32x32x16_bf16 v[176:191], v[196:199], v[60:63], v[176:191]\n" \
  "s_and_b32 s61, s61, 0xffff\n" \
  "v_exp_f32 v163, v163\n" \
  "v_exp_f32 v164, v164\n" \
  "v_exp_f32 v165, v165\n" \
  "v_smfmac_f32_32x32x32_bf16 v[80:95], v[200:203], v[144:151], v30\n" \
  "v_exp_f32 v166, v166\n" \
  "v_exp_f32 v167, v167\n" \
  "v_exp_f32 v168, v168\n" \
  "v_smfmac_f32_32x32x32_bf16 v[80:95], v[204:207], v[152:159], v30\n" \
  "s_waitcnt vmcnt(8)\n" \
  "v_exp_f32 v169, v169\n" \
  "v_exp_f32 v170, v170\n" \
  "v_exp_f32 v171, v171\n" \
  "v_exp_f32 v172, v172\n" \
  "v_exp_f32 v173, v173\n" \
  "v_exp_f32 v174, v174\n" \
  "v_exp_f32 v175, v175\n" \
  "v_mfma_f32_32x32x16_bf16 v[128:143], v[208:211], v[32:35], 0\n" \
  "v_exp_f32 v176, v176\n" \
  "v_exp_f32 v177, v177\n" \
  "v_exp_f32 v178, v178\n" \
  "v_mfma_f32_32x32x16_bf16 v[128:143], v[212:215], v[36:39], v[128:143]\n" \
  "v_exp_f32 v179, v179\n" \
  "v_exp_f32 v180, v180\n" \
  "v_exp_f32 v181, v181\n" \
  "v_smfmac_f32_32x32x32_bf16 v[96:111], v[200:203], v[160:167], v30\n" \
  "v_exp_f32 v182, v182\n" \
  "v_exp_f32 v183, v183\n" \
  "v_exp_f32 v184, v184\n" \
  "v_smfmac_f32_32x32x32_bf16 v[96:111], v[204:207], v[168:175], v30\n" \
  "v_exp_f32 v185, v185\n" \
  "v_exp_f32 v186, v186\n" \
  "v_exp_f32 v187, v187\n" \
  "v_exp_f32 v188, v188\n" \
  "v_exp_f32 v189, v189\n" \
  "v_exp_f32 v190, v190\n" \
  "v_exp_f32 v191, v191\n" \
  "v_mfma_f32_32x32x16_bf16 v[144:159], v[208:211], v[40:43], 0\n" \
  "v_exp_f32 v128, v128\n" \
  "v_exp_f32 v129, v129\n" \
  "v_exp_f32 v130, v130\n" \
  "v_mfma_f32_32x32x16_bf16 v[144:159], v[212:215], v[44:47], v[144:159]\n" \
  "v_exp_f32 v131, v131\n" \
  "v_exp_f32 v132, v132\n" \
  "v_exp_f32 v133, v133\n" \
  "v_smfmac_f32_32x32x32_bf16 v[112:127], v[200:203], v[176:183], v30\n" \
  "v_exp_f32 v134, v134\n" \
  "v_exp_f32 v135, v135\n" \
  "v_exp_f32 v136, v136\n" \
  "v_smfmac_f32_32x32x32_bf16 v[112:127], v[204:207], v[184:191], v30\n" \
  "v_exp_f32 v137, v137\n" \
  "v_exp_f32 v138, v138\n" \
  "v_exp_f32 v139, v139\n" \
  "v_exp_f32 v140, v140\n" \
  "v_exp_f32 v141, v141\n" \
  "v_exp_f32 v142, v142\n" \
  "v_exp_f32 v143, v143\n" \
  "v_mfma_f32_32x32x16_bf16 v[160:175], v[208:211], v[48:51], 0\n" \
  "v_exp_f32 v144, v144\n" \
  "v_exp_f32 v145, v145\n" \
  "v_exp_f32 v146, v146\n" \
  "v_mfma_f32_32x32x16_bf16 v[160:175], v[212:215], v[52:55], v[160:175]\n" \
  "v_exp_f32 v147, v147\n" \
  "v_exp_f32 v148, v148\n" \
  "v_exp_f32 v149, v149\n" \
  "v_smfmac_f32_32x32x32_bf16 v[64:79], v[216:219], v[128:135], v30\n" \
  "s_mov_b64 exec, s[32:33]\n" \
  "buffer_load_dwordx4 v[200:203], v16, s[36:39], s61 offen offset:1792 sc1\n" \
  "buffer_load_dwordx4 v[204:207], v16, s[36:39], s61 offen offset:2688 sc1\n" \
  "s_mov_b64 exec, -1\n" \
  "v_exp_f32 v150, v150\n" \
  "v_exp_f32 v151, v151\n" \
  "v_exp_f32 v152, v152\n" \
  "v_smfmac_f32_32x32x32_bf16 v[64:79], v[220:223], v[136:143], v30\n" \
  "buffer_load_dwordx4 v[192:195], v31, s[36:39], s61 offen sc1\n" \
  "buffer_load_dwordx4 v[196:199], v10, s[36:39], s61 offen offset:1280 sc1\n" \
  "v_exp_f32 v153, v153\n" \
  "v_exp_f32 v154, v154\n" \
  "v_exp_f32 v155, v155\n" \
  "v_exp_f32 v156, v156\n" \
  "v_exp_f32 v157, v157\n" \
  "v_exp_f32 v158, v158\n" \
  "v_exp_f32 v159, v159\n" \
  "v_mfma_f32_32x32x16_bf16 v[176:191], v[208:211], v[56:59], 0\n" \
  "s_add_u32 s61, s61, 4096\n" \
  "v_exp_f32 v160, v160\n" \
  "v_exp_f32 v161, v161\n" \
  "v_exp_f32 v162, v162\n" \
  "v_mfma_f32_32x32x16_bf16 v[176:191], v[212:215], v[60:63], v[176:191]\n" \
  "s_and_b32 s61, s61, 0xffff\n" \
  "v_exp_f32 v163, v163\n" \
  "v_exp_f32 v164, v164\n" \
  "v_exp_f32 v165, v165\n" \
  "v_smfmac_f32_32x32x32_bf16 v[80:95], v[216:219], v[144:151], v30\n" \
  "v_exp_f32 v166, v166\n" \
  "v_exp_f32 v167, v167\n" \
  "v_exp_f32 v168, v168\n" \
  "v_smfmac_f32_32x32x32_bf16 v[80:95], v[220:223], v[152:159], v30\n" \
  "s_waitcnt vmcnt(8)\n" \
  "v_exp_f32 v169, v169\n" \
  "v_exp_f32 v170, v170\n" \
  "v_exp_f32 v171, v171\n" \
  "v_exp_f32 v172, v172\n" \
  "v_exp_f32 v173, v173\n" \
  "v_exp_f32 v174, v174\n" \
  "v_exp_f32 v175, v175\n" \
  "v_mfma_f32_32x32x16_bf16 v[128:143], v[224:227], v[32:35], 0\n" \
  "v_exp_f32 v176, v176\n" \
  "v_exp_f32 v177, v177\n" \
  "v_exp_f32 v178, v178\n" \
  "v_mfma_f32_32x32x16_bf16 v[128:143], v[228:231], v[36:39], v[128:143]\n" \
  "v_exp_f32 v179, v179\n" \
  "v_exp_f32 v180, v180\n" \
  "v_exp_f32 v181, v181\n" \
  "v_smfmac_f32_32x32x32_bf16 v[96:111], v[216:219], v[160:167], v30\n" \
  "v_exp_f32 v182, v182\n" \
  "v_exp_f32 v183, v183\n" \
  "v_exp_f32 v184, v184\n" \
  "v_smfmac_f32_32x32x32_bf16 v[96:111], v[220:223], v[168:175], v30\n" \
  "v_exp_f32 v185, v185\n" \
  "v_exp_f32 v186, v186\n" \
  "v_exp_f32 v187, v187\n" \
  "v_exp_f32 v188, v188\n" \
  "v_exp_f32 v189, v189\n" \
  "v_exp_f32 v190, v190\n" \
  "v_exp_f32 v191, v191\n" \
  "v_mfma_f32_32x32x16_bf16 v[144:159], v[224:227], v[40:43], 0\n" \
  "v_exp_f32 v128, v128\n" \
  "v_exp_f32 v129, v129\n" \
  "v_exp_f32 v130, v130\n" \
  "v_mfma_f32_32x32x16_bf16 v[144:159], v[228:231], v[44:47], v[144:159]\n" \
  "v_exp_f32 v131, v131\n" \
  "v_exp_f32 v132, v132\n" \
  "v_exp_f32 v133, v133\n" \
  "v_smfmac_f32_32x32x32_bf16 v[112:127], v[216:219], v[176:183], v30\n" \
  "v_exp_f32 v134, v134\n" \
  "v_exp_f32 v135, v135\n" \
  "v_exp_f32 v136, v136\n" \
  "v_smfmac_f32_32x32x32_bf16 v[112:127], v[220:223], v[184:191], v30\n" \
  "v_exp_f32 v137, v137\n" \
  "v_exp_f32 v138, v138\n" \
  "v_exp_f32 v139, v139\n" \
  "v_exp_f32 v140, v140\n" \
  "v_exp_f32 v141, v141\n" \
  "v_exp_f32 v142, v142\n" \
  "v_exp_f32 v143, v143\n" \
  "v_mfma_f32_32x32x16_bf16 v[160:175], v[224:227], v[48:51], 0\n" \
  "v_exp_f32 v144, v144\n" \
  "v_exp_f32 v145, v145\n" \
  "v_exp_f32 v146, v146\n" \
  "v_mfma_f32_32x32x16_bf16 v[160:175], v[228:231], v[52:55], v[160:175]\n" \
  "v_exp_f32 v147, v147\n" \
  "v_exp_f32 v148, v148\n" \
  "v_exp_f32 v149, v149\n" \
  "v_smfmac_f32_32x32x32_bf16 v[64:79], v[232:235], v[128:135], v30\n" \
  "s_mov_b64 exec, s[32:33]\n" \
  "buffer_load_dwordx4 v[216:219], v16, s[36:39], s61 offen offset:1792 sc1\n" \
  "buffer_load_dwordx4 v[220:223], v16, s[36:39], s61 offen offset:2688 sc1\n" \
  "s_mov_b64 exec, -1\n" \
  "v_exp_f32 v150, v150\n" \
  "v_exp_f32 v151, v151\n" \
  "v_exp_f32 v152, v152\n" \
  "v_smfmac_f32_32x32x32_bf16 v[64:79], v[236:239], v[136:143], v30\n" \
  "buffer_load_dwordx4 v[208:211], v31, s[36:39], s61 offen sc1\n" \
  "buffer_load_dwordx4 v[212:215], v10, s[36:39], s61 offen offset:1280 sc1\n" \
  "v_exp_f32 v153, v153\n" \
  "v_exp_f32 v154, v154\n" \
  "v_exp_f32 v155, v155\n" \
  "v_exp_f32 v156, v156\n" \
  "v_exp_f32 v157, v157\n" \
  "v_exp_f32 v158, v158\n" \
  "v_exp_f32 v159, v159\n" \
  "v_mfma_f32_32x32x16_bf16 v[176:191], v[224:227], v[56:59], 0\n" \
  "s_add_u32 s61, s61, 4096\n" \
  "v_exp_f32 v160, v160\n" \
  "v_exp_f32 v161, v161\n" \
  "v_exp_f32 v162, v162\n" \
  "v_mfma_f32_32x32x16_bf16 v[176:191], v[228:231], v[60:63], v[176:191]\n" \
  "s_and_b32 s61, s61, 0xffff\n" \
  "v_exp_f32 v163, v163\n" \
  "v_exp_f32 v164, v164\n" \
  "v_exp_f32 v165, v165\n" \
  "v_smfmac_f32_32x32x32_bf16 v[80:95], v[232:235], v[144:151], v30\n" \
  "v_exp_f32 v166, v166\n" \
  "v_exp_f32 v167, v167\n" \
  "v_exp_f32 v168, v168\n" \
  "v_smfmac_f32_32x32x32_bf16 v[80:95], v[236:239], v[152:159], v30\n" \
  "s_waitcnt vmcnt(8)\n" \
  "v_exp_f32 v169, v169\n" \
  "v_exp_f32 v170, v170\n" \
  "v_exp_f32 v171, v171\n" \
  "v_exp_f32 v172, v172\n" \
  "v_exp_f32 v173, v173\n" \
  "v_exp_f32 v174, v174\n" \
  "v_exp_f32 v175, v175\n" \
  "v_mfma_f32_32x32x16_bf16 v[128:143], v[240:243], v[32:35], 0\n" \
  "v_exp_f32 v176, v176\n" \
  "v_exp_f32 v177, v177\n" \
  "v_exp_f32 v178, v178\n" \
  "v_mfma_f32_32x32x16_bf16 v[128:143], v[244:247], v[36:39], v[128:143]\n" \
  "v_exp_f32 v179, v179\n" \
  "v_exp_f32 v180, v180\n" \
  "v_exp_f32 v181, v181\n" \
  "v_smfmac_f32_32x32x32_bf16 v[96:111], v[232:235], v[160:167], v30\n" \
  "v_exp_f32 v182, v182\n" \
  "v_exp_f32 v183, v183\n" \
  "v_exp_f32 v184, v184\n" \
  "v_smfmac_f32_32x32x32_bf16 v[96:111], v[236:239], v[168:175], v30\n" \
  "v_exp_f32 v185, v185\n" \
  "v_exp_f32 v186, v186\n" \
  "v_exp_f32 v187, v187\n" \
  "v_exp_f32 v188, v188\n" \
  "v_exp_f32 v189, v189\n" \
  "v_exp_f32 v190, v190\n" \
  "v_exp_f32 v191, v191\n" \
  "v_mfma_f32_32x32x16_bf16 v[144:159], v[240:243], v[40:43], 0\n" \
  "v_exp_f32 v128, v128\n" \
  "v_exp_f32 v129, v129\n" \
  "v_exp_f32 v130, v130\n" \
  "v_mfma_f32_32x32x16_bf16 v[144:159], v[244:247], v[44:47], v[144:159]\n" \
  "v_exp_f32 v131, v131\n" \
  "v_exp_f32 v132, v132\n" \
  "v_exp_f32 v133, v133\n" \
  "v_smfmac_f32_32x32x32_bf16 v[112:127], v[232:235], v[176:183], v30\n" \
  "v_exp_f32 v134, v134\n" \
  "v_exp_f32 v135, v135\n" \
  "v_exp_f32 v136, v136\n" \
  "v_smfmac_f32_32x32x32_bf16 v[112:127], v[236:239], v[184:191], v30\n" \
  "v_exp_f32 v137, v137\n" \
  "v_exp_f32 v138, v138\n" \
  "v_exp_f32 v139, v139\n" \
  "v_exp_f32 v140, v140\n" \
  "v_exp_f32 v141, v141\n" \
  "v_exp_f32 v142, v142\n" \
  "v_exp_f32 v143, v143\n" \
  "v_mfma_f32_32x32x16_bf16 v[160:175], v[240:243], v[48:51], 0\n" \
  "v_exp_f32 v144, v144\n" \
  "v_exp_f32 v145, v145\n" \
  "v_exp_f32 v146, v146\n" \
  "v_mfma_f32_32x32x16_bf16 v[160:175], v[244:247], v[52:55], v[160:175]\n" \
  "v_exp_f32 v147, v147\n" \
  "v_exp_f32 v148, v148\n" \
  "v_exp_f32 v149, v149\n" \
  "v_smfmac_f32_32x32x32_bf16 v[64:79], v[248:251], v[128:135], v30\n" \
  "s_mov_b64 exec, s[32:33]\n" \
  "buffer_load_dwordx4 v[232:235], v16, s[36:39], s61 offen offset:1792 sc1\n" \
  "buffer_load_dwordx4 v[236:239], v16, s[36:39], s61 offen offset:2688 sc1\n" \
  "s_mov_b64 exec, -1\n" \
  "v_exp_f32 v150, v150\n" \
  "v_exp_f32 v151, v151\n" \
  "v_exp_f32 v152, v152\n" \
  "v_smfmac_f32_32x32x32_bf16 v[64:79], v[252:255], v[136:143], v30\n" \
  "buffer_load_dwordx4 v[224:227], v31, s[36:39], s61 offen sc1\n" \
  "buffer_load_dwordx4 v[228:231], v10, s[36:39], s61 offen offset:1280 sc1\n" \
  "v_exp_f32 v153, v153\n" \
  "v_exp_f32 v154, v154\n" \
  "v_exp_f32 v155, v155\n" \
  "v_exp_f32 v156, v156\n" \
  "v_exp_f32 v157, v157\n" \
  "v_exp_f32 v158, v158\n" \
  "v_exp_f32 v159, v159\n" \
  "v_mfma_f32_32x32x16_bf16 v[176:191], v[240:243], v[56:59], 0\n" \
  "s_add_u32 s61, s61, 4096\n" \
  "v_exp_f32 v160, v160\n" \
  "v_exp_f32 v161, v161\n" \
  "v_exp_f32 v162, v162\n" \
  "v_mfma_f32_32x32x16_bf16 v[176:191], v[244:247], v[60:63], v[176:191]\n" \
  "s_and_b32 s61, s61, 0xffff\n" \
  "v_exp_f32 v163, v163\n" \
  "v_exp_f32 v164, v164\n" \
  "v_exp_f32 v165, v165\n" \
  "v_smfmac_f32_32x32x32_bf16 v[80:95], v[248:251], v[144:151], v30\n" \
  "v_exp_f32 v166, v166\n" \
  "v_exp_f32 v167, v167\n" \
  "v_exp_f32 v168, v168\n" \
  "v_smfmac_f32_32x32x32_bf16 v[80:95], v[252:255], v[152:159], v30\n" \
  "s_waitcnt vmcnt(8)\n" \
  "v_exp_f32 v169, v169\n" \
  "v_exp_f32 v170, v170\n" \
  "v_exp_f32 v171, v171\n" \
  "v_exp_f32 v172, v172\n" \
  "v_exp_f32 v173, v173\n" \
  "v_exp_f32 v174, v174\n" \
  "v_exp_f32 v175, v175\n" \
  "v_mfma_f32_32x32x16_bf16 v[128:143], v[192:195], v[32:35], 0\n" \
  "v_exp_f32 v176, v176\n" \
  "v_exp_f32 v177, v177\n" \
  "v_exp_f32 v178, v178\n" \
  "v_mfma_f32_32x32x16_bf16 v[128:143], v[196:199], v[36:39], v[128:143]\n" \
  "v_exp_f32 v179, v179\n" \
  "v_exp_f32 v180, v180\n" \
  "v_exp_f32 v181, v181\n" \
  "v_smfmac_f32_32x32x32_bf16 v[96:111], v[248:251], v[160:167], v30\n" \
  "v_exp_f32 v182, v182\n" \
  "v_exp_f32 v183, v183\n" \
  "v_exp_f32 v184, v184\n" \
  "v_smfmac_f32_32x32x32_bf16 v[96:111], v[252:255], v[168:175], v30\n" \
  "v_exp_f32 v185, v185\n" \
  "v_exp_f32 v186, v186\n" \
  "v_exp_f32 v187, v187\n" \
  "v_exp_f32 v188, v188\n" \
  "v_exp_f32 v189, v189\n" \
  "v_exp_f32 v190, v190\n" \
  "v_exp_f32 v191, v191\n" \
  "v_mfma_f32_32x32x16_bf16 v[144:159], v[192:195], v[40:43], 0\n" \
  "v_exp_f32 v128, v128\n" \
  "v_exp_f32 v129, v129\n" \
  "v_exp_f32 v130, v130\n" \
  "v_mfma_f32_32x32x16_bf16 v[144:159], v[196:199], v[44:47], v[144:159]\n" \
  "v_exp_f32 v131, v131\n" \
  "v_exp_f32 v132, v132\n" \
  "v_exp_f32 v133, v133\n" \
  "v_smfmac_f32_32x32x32_bf16 v[112:127], v[248:251], v[176:183], v30\n" \
  "v_exp_f32 v134, v134\n" \
  "v_exp_f32 v135, v135\n" \
  "v_exp_f32 v136, v136\n" \
  "v_smfmac_f32_32x32x32_bf16 v[112:127], v[252:255], v[184:191], v30\n" \
  "v_exp_f32 v137, v137\n" \
  "v_exp_f32 v138, v138\n" \
  "v_exp_f32 v139, v139\n" \
  "v_exp_f32 v140, v140\n" \
  "v_exp_f32 v141, v141\n" \
  "v_exp_f32 v142, v142\n" \
  "v_exp_f32 v143, v143\n" \
  "v_mfma_f32_32x32x16_bf16 v[160:175], v[192:195], v[48:51], 0\n" \
  "v_exp_f32 v144, v144\n" \
  "v_exp_f32 v145, v145\n" \
  "v_exp_f32 v146, v146\n" \
  "v_mfma_f32_32x32x16_bf16 v[160:175], v[196:199], v[52:55], v[160:175]\n" \
  "v_exp_f32 v147, v147\n" \
  "v_exp_f32 v148, v148\n" \
  "v_exp_f32 v149, v149\n" \
  "v_smfmac_f32_32x32x32_bf16 v[64:79], v[200:203], v[128:135], v30\n" \
  "s_mov_b64 exec, s[32:33]\n" \
  "buffer_load_dwordx4 v[248:251], v16, s[36:39], s61 offen offset:1792 sc1\n" \
  "buffer_load_dwordx4 v[252:255], v16, s[36:39], s61 offen offset:2688 sc1\n" \
  "s_mov_b64 exec, -1\n" \
  "v_exp_f32 v150, v150\n" \
  "v_exp_f32 v151, v151\n" \
  "v_exp_f32 v152, v152\n" \
  "v_smfmac_f32_32x32x32_bf16 v[64:79], v[204:207], v[136:143], v30\n" \
  "buffer_load_dwordx4 v[240:243], v31, s[36:39], s61 offen sc1\n" \
  "buffer_load_dwordx4 v[244:247], v10, s[36:39], s61 offen offset:1280 sc1\n" \
  "v_exp_f32 v153, v153\n" \
  "v_exp_f32 v154, v154\n" \
  "v_exp_f32 v155, v155\n" \
  "v_exp_f32 v156, v156\n" \
  "v_exp_f32 v157, v157\n" \
  "v_exp_f32 v158, v158\n" \
  "v_exp_f32 v159, v159\n" \
  "v_mfma_f32_32x32x16_bf16 v[176:191], v[192:195], v[56:59], 0\n" \
  "s_add_u32 s61, s61, 4096\n" \
  "v_exp_f32 v160, v160\n" \
  "v_exp_f32 v161, v161\n" \
  "v_exp_f32 v162, v162\n" \
  "v_mfma_f32_32x32x16_bf16 v[176:191], v[196:199], v[60:63], v[176:191]\n" \
  "s_and_b32 s61, s61, 0xffff\n" \
  "v_exp_f32 v163, v163\n" \
  "v_exp_f32 v164, v164\n" \
  "v_exp_f32 v165, v165\n" \
  "v_smfmac_f32_32x32x32_bf16 v[80:95], v[200:203], v[144:151], v30\n" \
  "v_exp_f32 v166, v166\n" \
  "v_exp_f32 v167, v167\n" \
  "v_exp_f32 v168, v168\n" \
  "v_smfmac_f32_32x32x32_bf16 v[80:95], v[204:207], v[152:159], v30\n" \
  "s_waitcnt vmcnt(8)\n" \
  "v_exp_f32 v169, v169\n" \
  "v_exp_f32 v170, v170\n" \
  "v_exp_f32 v171, v171\n" \
  "v_exp_f32 v172, v172\n" \
  "v_exp_f32 v173, v173\n" \
  "v_exp_f32 v174, v174\n" \
  "v_exp_f32 v175, v175\n" \
  "v_mfma_f32_32x32x16_bf16 v[128:143], v[208:211], v[32:35], 0\n" \
  "v_exp_f32 v176, v176\n" \
  "v_exp_f32 v177, v177\n" \
  "v_exp_f32 v178, v178\n" \
  "v_mfma_f32_32x32x16_bf16 v[128:143], v[212:215], v[36:39], v[128:143]\n" \
  "v_exp_f32 v179, v179\n" \
  "v_exp_f32 v180, v180\n" \
  "v_exp_f32 v181, v181\n" \
  "v_smfmac_f32_32x32x32_bf16 v[96:111], v[200:203], v[160:167], v30\n" \
  "v_exp_f32 v182, v182\n" \
  "v_exp_f32 v183, v183\n" \
  "v_exp_f32 v184, v184\n" \
  "v_smfmac_f32_32x32x32_bf16 v[96:111], v[204:207], v[168:175], v30\n" \
  "v_exp_f32 v185, v185\n" \
  "v_exp_f32 v186, v186\n" \
  "v_exp_f32 v187, v187\n" \
  "v_exp_f32 v188, v188\n" \
  "v_exp_f32 v189, v189\n" \
  "v_exp_f32 v190, v190\n" \
  "v_exp_f32 v191, v191\n" \
  "v_mfma_f32_32x32x16_bf16 v[144:159], v[208:211], v[40:43], 0\n" \
  "v_exp_f32 v128, v128\n" \
  "v_exp_f32 v129, v129\n" \
  "v_exp_f32 v130, v130\n" \
  "v_mfma_f32_32x32x16_bf16 v[144:159], v[212:215], v[44:47], v[144:159]\n" \
  "v_exp_f32 v131, v131\n" \
  "v_exp_f32 v132, v132\n" \
  "v_exp_f32 v133, v133\n" \
  "v_smfmac_f32_32x32x32_bf16 v[112:127], v[200:203], v[176:183], v30\n" \
  "v_exp_f32 v134, v134\n" \
  "v_exp_f32 v135, v135\n" \
  "v_exp_f32 v136, v136\n" \
  "v_smfmac_f32_32x32x32_bf16 v[112:127], v[204:207], v[184:191], v30\n" \
  "v_exp_f32 v137, v137\n" \
  "v_exp_f32 v138, v138\n" \
  "v_exp_f32 v139, v139\n" \
  "v_exp_f32 v140, v140\n" \
  "v_exp_f32 v141, v141\n" \
  "v_exp_f32 v142, v142\n" \
  "v_exp_f32 v143, v143\n" \
  "v_mfma_f32_32x32x16_bf16 v[160:175], v[208:211], v[48:51], 0\n" \
  "v_exp_f32 v144, v144\n" \
  "v_exp_f32 v145, v145\n" \
  "v_exp_f32 v146, v146\n" \
  "v_mfma_f32_32x32x16_bf16 v[160:175], v[212:215], v[52:55], v[160:175]\n" \
  "v_exp_f32 v147, v147\n" \
  "v_exp_f32 v148, v148\n" \
  "v_exp_f32 v149, v149\n" \
  "v_smfmac_f32_32x32x32_bf16 v[64:79], v[216:219], v[128:135], v30\n" \
  "s_mov_b64 exec, s[32:33]\n" \
  "buffer_load_dwordx4 v[200:203], v16, s[36:39], s61 offen offset:1792 sc1\n" \
  "buffer_load_dwordx4 v[204:207], v16, s[36:39], s61 offen offset:2688 sc1\n" \
  "s_mov_b64 exec, -1\n" \
  "v_exp_f32 v150, v150\n" \
  "v_exp_f32 v151, v151\n" \
  "v_exp_f32 v152, v152\n" \
  "v_smfmac_f32_32x32x32_bf16 v[64:79], v[220:223], v[136:143], v30\n" \
  "buffer_load_dwordx4 v[192:195], v31, s[36:39], s61 offen sc1\n" \
  "buffer_load_dwordx4 v[196:199], v10, s[36:39], s61 offen offset:1280 sc1\n" \
  "v_exp_f32 v153, v153\n" \
  "v_exp_f32 v154, v154\n" \
  "v_exp_f32 v155, v155\n" \
  "v_exp_f32 v156, v156\n" \
  "v_exp_f32 v157, v157\n" \
  "v_exp_f32 v158, v158\n" \
  "v_exp_f32 v159, v159\n" \
  "v_mfma_f32_32x32x16_bf16 v[176:191], v[208:211], v[56:59], 0\n" \
  "s_add_u32 s61, s61, 4096\n" \
  "v_exp_f32 v160, v160\n" \
  "v_exp_f32 v161, v161\n" \
  "v_exp_f32 v162, v162\n" \
  "v_mfma_f32_32x32x16_bf16 v[176:191], v[212:215], v[60:63], v[176:191]\n" \
  "s_and_b32 s61, s61, 0xffff\n" \
  "v_exp_f32 v163, v163\n" \
  "v_exp_f32 v164, v164\n" \
  "v_exp_f32 v165, v165\n" \
  "v_smfmac_f32_32x32x32_bf16 v[80:95], v[216:219], v[144:151], v30\n" \
  "v_exp_f32 v166, v166\n" \
  "v_exp_f32 v167, v167\n" \
  "v_exp_f32 v168, v168\n" \
  "v_smfmac_f32_32x32x32_bf16 v[80:95], v[220:223], v[152:159], v30\n" \
  "s_waitcnt vmcnt(8)\n" \
  "v_exp_f32 v169, v169\n" \
  "v_exp_f32 v170, v170\n" \
  "v_exp_f32 v171, v171\n" \
  "v_exp_f32 v172, v172\n" \
  "v_exp_f32 v173, v173\n" \
  "v_exp_f32 v174, v174\n" \
  "v_exp_f32 v175, v175\n" \
  "v_mfma_f32_32x32x16_bf16 v[128:143], v[224:227], v[32:35], 0\n" \
  "v_exp_f32 v176, v176\n" \
  "v_exp_f32 v177, v177\n" \
  "v_exp_f32 v178, v178\n" \
  "v_mfma_f32_32x32x16_bf16 v[128:143], v[228:231], v[36:39], v[128:143]\n" \
  "v_exp_f32 v179, v179\n" \
  "v_exp_f32 v180, v180\n" \
  "v_exp_f32 v181, v181\n" \
  "v_smfmac_f32_32x32x32_bf16 v[96:111], v[216:219], v[160:167], v30\n" \
  "v_exp_f32 v182, v182\n" \
  "v_exp_f32 v183, v183\n" \
  "v_exp_f32 v184, v184\n" \
  "v_smfmac_f32_32x32x32_bf16 v[96:111], v[220:223], v[168:175], v30\n" \
  "v_exp_f32 v185, v185\n" \
  "v_exp_f32 v186, v186\n" \
  "v_exp_f32 v187, v187\n" \
  "v_exp_f32 v188, v188\n" \
  "v_exp_f32 v189, v189\n" \
  "v_exp_f32 v190, v190\n" \
  "v_exp_f32 v191, v191\n" \
  "v_mfma_f32_32x32x16_bf16 v[144:159], v[224:227], v[40:43], 0\n" \
  "v_exp_f32 v128, v128\n" \
  "v_exp_f32 v129, v129\n" \
  "v_exp_f32 v130, v130\n" \
  "v_mfma_f32_32x32x16_bf16 v[144:159], v[228:231], v[44:47], v[144:159]\n" \
  "v_exp_f32 v131, v131\n" \
  "v_exp_f32 v132, v132\n" \
  "v_exp_f32 v133, v133\n" \
  "v_smfmac_f32_32x32x32_bf16 v[112:127], v[216:219], v[176:183], v30\n" \
  "v_exp_f32 v134, v134\n" \
  "v_exp_f32 v135, v135\n" \
  "v_exp_f32 v136, v136\n" \
  "v_smfmac_f32_32x32x32_bf16 v[112:127], v[220:223], v[184:191], v30\n" \
  "v_exp_f32 v137, v137\n" \
  "v_exp_f32 v138, v138\n" \
  "v_exp_f32 v139, v139\n" \
  "v_exp_f32 v140, v140\n" \
  "v_exp_f32 v141, v141\n" \
  "v_exp_f32 v142, v142\n" \
  "v_exp_f32 v143, v143\n" \
  "v_mfma_f32_32x32x16_bf16 v[160:175], v[224:227], v[48:51], 0\n" \
  "v_exp_f32 v144, v144\n" \
  "v_exp_f32 v145, v145\n" \
  "v_exp_f32 v146, v146\n" \
  "v_mfma_f32_32x32x16_bf16 v[160:175], v[228:231], v[52:55], v[160:175]\n" \
  "v_exp_f32 v147, v147\n" \
  "v_exp_f32 v148, v148\n" \
  "v_exp_f32 v149, v149\n" \
  "v_smfmac_f32_32x32x32_bf16 v[64:79], v[232:235], v[128:135], v30\n" \
  "s_mov_b64 exec, s[32:33]\n" \
  "buffer_load_dwordx4 v[216:219], v16, s[36:39], s61 offen offset:1792 sc1\n" \
  "buffer_load_dwordx4 v[220:223], v16, s[36:39], s61 offen offset:2688 sc1\n" \
  "s_mov_b64 exec, -1\n" \
  "v_exp_f32 v150, v150\n" \
  "v_exp_f32 v151, v151\n" \
  "v_exp_f32 v152, v152\n" \
  "v_smfmac_f32_32x32x32_bf16 v[64:79], v[236:239], v[136:143], v30\n" \
  "buffer_load_dwordx4 v[208:211], v31, s[36:39], s61 offen sc1\n" \
  "buffer_load_dwordx4 v[212:215], v10, s[36:39], s61 offen offset:1280 sc1\n" \
  "v_exp_f32 v153, v153\n" \
  "v_exp_f32 v154, v154\n" \
  "v_exp_f32 v155, v155\n" \
  "v_exp_f32 v156, v156\n" \
  "v_exp_f32 v157, v157\n" \
  "v_exp_f32 v158, v158\n" \
  "v_exp_f32 v159, v159\n" \
  "v_mfma_f32_32x32x16_bf16 v[176:191], v[224:227], v[56:59], 0\n" \
  "s_add_u32 s61, s61, 4096\n" \
  "v_exp_f32 v160, v160\n" \
  "v_exp_f32 v161, v161\n" \
  "v_exp_f32 v162, v162\n" \
  "v_mfma_f32_32x32x16_bf16 v[176:191], v[228:231], v[60:63], v[176:191]\n" \
  "s_and_b32 s61, s61, 0xffff\n" \
  "v_exp_f32 v163, v163\n" \
  "v_exp_f32 v164, v164\n" \
  "v_exp_f32 v165, v165\n" \
  "v_smfmac_f32_32x32x32_bf16 v[80:95], v[232:235], v[144:151], v30\n" \
  "v_exp_f32 v166, v166\n" \
  "v_exp_f32 v167, v167\n" \
  "v_exp_f32 v168, v168\n" \
  "v_smfmac_f32_32x32x32_bf16 v[80:95], v[236:239], v[152:159], v30\n" \
  "s_waitcnt vmcnt(8)\n" \
  "v_exp_f32 v169, v169\n" \
  "v_exp_f32 v170, v170\n" \
  "v_exp_f32 v171, v171\n" \
  "v_exp_f32 v172, v172\n" \
  "v_exp_f32 v173, v173\n" \
  "v_exp_f32 v174, v174\n" \
  "v_exp_f32 v175, v175\n" \
  "v_mfma_f32_32x32x16_bf16 v[128:143], v[240:243], v[32:35], 0\n" \
  "v_exp_f32 v176, v176\n" \
  "v_exp_f32 v177, v177\n" \
  "v_exp_f32 v178, v178\n" \
  "v_mfma_f32_32x32x16_bf16 v[128:143], v[244:247], v[36:39], v[128:143]\n" \
  "v_exp_f32 v179, v179\n" \
  "v_exp_f32 v180, v180\n" \
  "v_exp_f32 v181, v181\n" \
  "v_smfmac_f32_32x32x32_bf16 v[96:111], v[232:235], v[160:167], v30\n" \
  "v_exp_f32 v182, v182\n" \
  "v_exp_f32 v183, v183\n" \
  "v_exp_f32 v184, v184\n" \
  "v_smfmac_f32_32x32x32_bf16 v[96:111], v[236:239], v[168:175], v30\n" \
  "v_exp_f32 v185, v185\n" \
  "v_exp_f32 v186, v186\n" \
  "v_exp_f32 v187, v187\n" \
  "v_exp_f32 v188, v188\n" \
  "v_exp_f32 v189, v189\n" \
  "v_exp_f32 v190, v190\n" \
  "v_exp_f32 v191, v191\n" \
  "v_mfma_f32_32x32x16_bf16 v[144:159], v[240:243], v[40:43], 0\n" \
  "v_exp_f32 v128, v128\n" \
  "v_exp_f32 v129, v129\n" \
  "v_exp_f32 v130, v130\n" \
  "v_mfma_f32_32x32x16_bf16 v[144:159], v[244:247], v[44:47], v[144:159]\n" \
  "v_exp_f32 v131, v131\n" \
  "v_exp_f32 v132, v132\n" \
  "v_exp_f32 v133, v133\n" \
  "v_smfmac_f32_32x32x32_bf16 v[112:127], v[232:235], v[176:183], v30\n" \
  "v_exp_f32 v134, v134\n" \
  "v_exp_f32 v135, v135\n" \
  "v_exp_f32 v136, v136\n" \
  "v_smfmac_f32_32x32x32_bf16 v[112:127], v[236:239], v[184:191], v30\n" \
  "v_exp_f32 v137, v137\n" \
  "v_exp_f32 v138, v138\n" \
  "v_exp_f32 v139, v139\n" \
  "v_exp_f32 v140, v140\n" \
  "v_exp_f32 v141, v141\n" \
  "v_exp_f32 v142, v142\n" \
  "v_exp_f32 v143, v143\n" \
  "v_mfma_f32_32x32x16_bf16 v[160:175], v[240:243], v[48:51], 0\n" \
  "v_exp_f32 v144, v144\n" \
  "v_exp_f32 v145, v145\n" \
  "v_exp_f32 v146, v146\n" \
  "v_mfma_f32_32x32x16_bf16 v[160:175], v[244:247], v[52:55], v[160:175]\n" \
  "v_exp_f32 v147, v147\n" \
  "v_exp_f32 v148, v148\n" \
  "v_exp_f32 v149, v149\n" \
  "v_smfmac_f32_32x32x32_bf16 v[64:79], v[248:251], v[128:135], v30\n" \
  "s_mov_b64 exec, s[32:33]\n" \
  "buffer_load_dwordx4 v[232:235], v16, s[36:39], s61 offen offset:1792 sc1\n" \
  "buffer_load_dwordx4 v[236:239], v16, s[36:39], s61 offen offset:2688 sc1\n" \
  "s_mov_b64 exec, -1\n" \
  "v_exp_f32 v150, v150\n" \
  "v_exp_f32 v151, v151\n" \
  "v_exp_f32 v152, v152\n" \
  "v_smfmac_f32_32x32x32_bf16 v[64:79], v[252:255], v[136:143], v30\n" \
  "buffer_load_dwordx4 v[224:227], v31, s[36:39], s61 offen sc1\n" \
  "buffer_load_dwordx4 v[228:231], v10, s[36:39], s61 offen offset:1280 sc1\n" \
  "v_exp_f32 v153, v153\n" \
  "v_exp_f32 v154, v154\n" \
  "v_exp_f32 v155, v155\n" \
  "v_exp_f32 v156, v156\n" \
  "v_exp_f32 v157, v157\n" \
  "v_exp_f32 v158, v158\n" \
  "v_exp_f32 v159, v159\n" \
  "v_mfma_f32_32x32x16_bf16 v[176:191], v[240:243], v[56:59], 0\n" \
  "s_add_u32 s61, s61, 4096\n" \
  "v_exp_f32 v160, v160\n" \
  "v_exp_f32 v161, v161\n" \
  "v_exp_f32 v162, v162\n" \
  "v_mfma_f32_32x32x16_bf16 v[176:191], v[244:247], v[60:63], v[176:191]\n" \
  "s_and_b32 s61, s61, 0xffff\n" \
  "v_exp_f32 v163, v163\n" \
  "v_exp_f32 v164, v164\n" \
  "v_exp_f32 v165, v165\n" \
  "v_smfmac_f32_32x32x32_bf16 v[80:95], v[248:251], v[144:151], v30\n" \
  "v_exp_f32 v166, v166\n" \
  "v_exp_f32 v167, v167\n" \
  "v_exp_f32 v168, v168\n" \
  "v_smfmac_f32_32x32x32_bf16 v[80:95], v[252:255], v[152:159], v30\n" \
  "s_waitcnt vmcnt(8)\n" \
  "v_exp_f32 v169, v169\n" \
  "v_exp_f32 v170, v170\n" \
  "v_exp_f32 v171, v171\n" \
  "v_exp_f32 v172, v172\n" \
  "v_exp_f32 v173, v173\n" \
  "v_exp_f32 v174, v174\n" \
  "v_exp_f32 v175, v175\n" \
  "v_mfma_f32_32x32x16_bf16 v[128:143], v[192:195], v[32:35], 0\n" \
  "v_exp_f32 v176, v176\n" \
  "v_exp_f32 v177, v177\n" \
  "v_exp_f32 v178, v178\n" \
  "v_mfma_f32_32x32x16_bf16 v[128:143], v[196:199], v[36:39], v[128:143]\n" \
  "v_exp_f32 v179, v179\n" \
  "v_exp_f32 v180, v180\n" \
  "v_exp_f32 v181, v181\n" \
  "v_smfmac_f32_32x32x32_bf16 v[96:111], v[248:251], v[160:167], v30\n" \
  "v_exp_f32 v182, v182\n" \
  "v_exp_f32 v183, v183\n" \
  "v_exp_f32 v184, v184\n" \
  "v_smfmac_f32_32x32x32_bf16 v[96:111], v[252:255], v[168:175], v30\n" \
  "v_exp_f32 v185, v185\n" \
  "v_exp_f32 v186, v186\n" \
  "v_exp_f32 v187, v187\n" \
  "v_exp_f32 v188, v188\n" \
  "v_exp_f32 v189, v189\n" \
  "v_exp_f32 v190, v190\n" \
  "v_exp_f32 v191, v191\n" \
  "v_mfma_f32_32x32x16_bf16 v[144:159], v[192:195], v[40:43], 0\n" \
  "v_exp_f32 v128, v128\n" \
  "v_exp_f32 v129, v129\n" \
  "v_exp_f32 v130, v130\n" \
  "v_mfma_f32_32x32x16_bf16 v[144:159], v[196:199], v[44:47], v[144:159]\n" \
  "v_exp_f32 v131, v131\n" \
  "v_exp_f32 v132, v132\n" \
  "v_exp_f32 v133, v133\n" \
  "v_smfmac_f32_32x32x32_bf16 v[112:127], v[248:251], v[176:183], v30\n" \
  "v_exp_f32 v134, v134\n" \
  "v_exp_f32 v135, v135\n" \
  "v_exp_f32 v136, v136\n" \
  "v_smfmac_f32_32x32x32_bf16 v[112:127], v[252:255], v[184:191], v30\n" \
  "v_exp_f32 v137, v137\n" \
  "v_exp_f32 v138, v138\n" \
  "v_exp_f32 v139, v139\n" \
  "v_exp_f32 v140, v140\n" \
  "v_exp_f32 v141, v141\n" \
  "v_exp_f32 v142, v142\n" \
  "v_exp_f32 v143, v143\n" \
  "v_mfma_f32_32x32x16_bf16 v[160:175], v[192:195], v[48:51], 0\n" \
  "v_exp_f32 v144, v144\n" \
  "v_exp_f32 v145, v145\n" \
  "v_exp_f32 v146, v146\n" \
  "v_mfma_f32_32x32x16_bf16 v[160:175], v[196:199], v[52:55], v[160:175]\n" \
  "v_exp_f32 v147, v147\n" \
  "v_exp_f32 v148, v148\n" \
  "v_exp_f32 v149, v149\n" \
  "v_smfmac_f32_32x32x32_bf16 v[64:79], v[200:203], v[128:135], v30\n" \
  "s_mov_b64 exec, s[32:33]\n" \
  "buffer_load_dwordx4 v[248:251], v16, s[36:39], s61 offen offset:1792 sc1\n" \
  "buffer_load_dwordx4 v[252:255], v16, s[36:39], s61 offen offset:2688 sc1\n" \
  "s_mov_b64 exec, -1\n" \
  "v_exp_f32 v150, v150\n" \
  "v_exp_f32 v151, v151\n" \
  "v_exp_f32 v152, v152\n" \
  "v_smfmac_f32_32x32x32_bf16 v[64:79], v[204:207], v[136:143], v30\n" \
  "buffer_load_dwordx4 v[240:243], v31, s[36:39], s61 offen sc1\n" \
  "buffer_load_dwordx4 v[244:247], v10, s[36:39], s61 offen offset:1280 sc1\n" \
  "v_exp_f32 v153, v153\n" \
  "v_exp_f32 v154, v154\n" \
  "v_exp_f32 v155, v155\n" \
  "v_exp_f32 v156, v156\n" \
  "v_exp_f32 v157, v157\n" \
  "v_exp_f32 v158, v158\n" \
  "v_exp_f32 v159, v159\n" \
  "v_mfma_f32_32x32x16_bf16 v[176:191], v[192:195], v[56:59], 0\n" \
  "s_add_u32 s61, s61, 4096\n" \
  "v_exp_f32 v160, v160\n" \
  "v_exp_f32 v161, v161\n" \
  "v_exp_f32 v162, v162\n" \
  "v_mfma_f32_32x32x16_bf16 v[176:191], v[196:199], v[60:63], v[176:191]\n" \
  "s_and_b32 s61, s61, 0xffff\n" \
  "v_exp_f32 v163, v163\n" \
  "v_exp_f32 v164, v164\n" \
  "v_exp_f32 v165, v165\n" \
  "v_smfmac_f32_32x32x32_bf16 v[80:95], v[200:203], v[144:151], v30\n" \
  "v_exp_f32 v166, v166\n" \
  "v_exp_f32 v167, v167\n" \
  "v_exp_f32 v168, v168\n" \
  "v_smfmac_f32_32x32x32_bf16 v[80:95], v[204:207], v[152:159], v30\n" \
  "s_waitcnt vmcnt(8)\n" \
  "v_exp_f32 v169, v169\n" \
  "v_exp_f32 v170, v170\n" \
  "v_exp_f32 v171, v171\n" \
  "v_exp_f32 v172, v172\n" \
  "v_exp_f32 v173, v173\n" \
  "v_exp_f32 v174, v174\n" \
  "v_exp_f32 v175, v175\n" \
  "v_mfma_f32_32x32x16_bf16 v[128:143], v[208:211], v[32:35], 0\n" \
  "v_exp_f32 v176, v176\n" \
  "v_exp_f32 v177, v177\n" \
  "v_exp_f32 v178, v178\n" \
  "v_mfma_f32_32x32x16_bf16 v[128:143], v[212:215], v[36:39], v[128:143]\n" \
  "v_exp_f32 v179, v179\n" \
  "v_exp_f32 v180, v180\n" \
  "v_exp_f32 v181, v181\n" \
  "v_smfmac_f32_32x32x32_bf16 v[96:111], v[200:203], v[160:167], v30\n" \
  "v_exp_f32 v182, v182\n" \
  "v_exp_f32 v183, v183\n" \
  "v_exp_f32 v184, v184\n" \
  "v_smfmac_f32_32x32x32_bf16 v[96:111], v[204:207], v[168:175], v30\n" \
  "v_exp_f32 v185, v185\n" \
  "v_exp_f32 v186, v186\n" \
  "v_exp_f32 v187, v187\n" \
  "v_exp_f32 v188, v188\n" \
  "v_exp_f32 v189, v189\n" \
  "v_exp_f32 v190, v190\n" \
  "v_exp_f32 v191, v191\n" \
  "v_mfma_f32_32x32x16_bf16 v[144:159], v[208:211], v[40:43], 0\n" \
  "v_exp_f32 v128, v128\n" \
  "v_exp_f32 v129, v129\n" \
  "v_exp_f32 v130, v130\n" \
  "v_mfma_f32_32x32x16_bf16 v[144:159], v[212:215], v[44:47], v[144:159]\n" \
  "v_exp_f32 v131, v131\n" \
  "v_exp_f32 v132, v132\n" \
  "v_exp_f32 v133, v133\n" \
  "v_smfmac_f32_32x32x32_bf16 v[112:127], v[200:203], v[176:183], v30\n" \
  "v_exp_f32 v134, v134\n" \
  "v_exp_f32 v135, v135\n" \
  "v_exp_f32 v136, v136\n" \
  "v_smfmac_f32_32x32x32_bf16 v[112:127], v[204:207], v[184:191], v30\n" \
  "v_exp_f32 v137, v137\n" \
  "v_exp_f32 v138, v138\n" \
  "v_exp_f32 v139, v139\n" \
  "v_exp_f32 v140, v140\n" \
  "v_exp_f32 v141, v141\n" \
  "v_exp_f32 v142, v142\n" \
  "v_exp_f32 v143, v143\n" \
  "v_mfma_f32_32x32x16_bf16 v[160:175], v[208:211], v[48:51], 0\n" \
  "v_exp_f32 v144, v144\n" \
  "v_exp_f32 v145, v145\n" \
  "v_exp_f32 v146, v146\n" \
  "v_mfma_f32_32x32x16_bf16 v[160:175], v[212:215], v[52:55], v[160:175]\n" \
  "v_exp_f32 v147, v147\n" \
  "v_exp_f32 v148, v148\n" \
  "v_exp_f32 v149, v149\n" \
  "v_smfmac_f32_32x32x32_bf16 v[64:79], v[216:219], v[128:135], v30\n" \
  "v_exp_f32 v150, v150\n" \
  "v_exp_f32 v151, v151\n" \
  "v_exp_f32 v152, v152\n" \
  "v_smfmac_f32_32x32x32_bf16 v[64:79], v[220:223], v[136:143], v30\n" \
  "v_exp_f32 v153, v153\n" \
  "v_exp_f32 v154, v154\n" \
  "v_exp_f32 v155, v155\n" \
  "v_exp_f32 v156, v156\n" \
  "v_exp_f32 v157, v157\n" \
  "v_exp_f32 v158, v158\n" \
  "v_exp_f32 v159, v159\n" \
  "v_mfma_f32_32x32x16_bf16 v[176:191], v[208:211], v[56:59], 0\n" \
  "v_exp_f32 v160, v160\n" \
  "v_exp_f32 v161, v161\n" \
  "v_exp_f32 v162, v162\n" \
  "v_mfma_f32_32x32x16_bf16 v[176:191], v[212:215], v[60:63], v[176:191]\n" \
  "v_exp_f32 v163, v163\n" \
  "v_exp_f32 v164, v164\n" \
  "v_exp_f32 v165, v165\n" \
  "v_smfmac_f32_32x32x32_bf16 v[80:95], v[216:219], v[144:151], v30\n" \
  "v_exp_f32 v166, v166\n" \
  "v_exp_f32 v167, v167\n" \
  "v_exp_f32 v168, v168\n" \
  "v_smfmac_f32_32x32x32_bf16 v[80:95], v[220:223], v[152:159], v30\n" \
  "s_waitcnt vmcnt(4)\n" \
  "v_exp_f32 v169, v169\n" \
  "v_exp_f32 v170, v170\n" \
  "v_exp_f32 v171, v171\n" \
  "v_exp_f32 v172, v172\n" \
  "v_exp_f32 v173, v173\n" \
  "v_exp_f32 v174, v174\n" \
  "v_exp_f32 v175, v175\n" \
  "v_mfma_f32_32x32x16_bf16 v[128:143], v[224:227], v[32:35], 0\n" \
  "v_exp_f32 v176, v176\n" \
  "v_exp_f32 v177, v177\n" \
  "v_exp_f32 v178, v178\n" \
  "v_mfma_f32_32x32x16_bf16 v[128:143], v[228:231], v[36:39], v[128:143]\n" \
  "v_exp_f32 v179, v179\n" \
  "v_exp_f32 v180, v180\n" \
  "v_exp_f32 v181, v181\n" \
  "v_smfmac_f32_32x32x32_bf16 v[96:111], v[216:219], v[160:167], v30\n" \
  "v_exp_f32 v182, v182\n" \
  "v_exp_f32 v183, v183\n" \
  "v_exp_f32 v184, v184\n" \
  "v_smfmac_f32_32x32x32_bf16 v[96:111], v[220:223], v[168:175], v30\n" \
  "v_exp_f32 v185, v185\n" \
  "v_exp_f32 v186, v186\n" \
  "v_exp_f32 v187, v187\n" \
  "v_exp_f32 v188, v188\n" \
  "v_exp_f32 v189, v189\n" \
  "v_exp_f32 v190, v190\n" \
  "v_exp_f32 v191, v191\n" \
  "v_mfma_f32_32x32x16_bf16 v[144:159], v[224:227], v[40:43], 0\n" \
  "v_exp_f32 v128, v128\n" \
  "v_exp_f32 v129, v129\n" \
  "v_exp_f32 v130, v130\n" \
  "v_mfma_f32_32x32x16_bf16 v[144:159], v[228:231], v[44:47], v[144:159]\n" \
  "v_exp_f32 v131, v131\n" \
  "v_exp_f32 v132, v132\n" \
  "v_exp_f32 v133, v133\n" \
  "v_smfmac_f32_32x32x32_bf16 v[112:127], v[216:219], v[176:183], v30\n" \
  "v_exp_f32 v134, v134\n" \
  "v_exp_f32 v135, v135\n" \
  "v_exp_f32 v136, v136\n" \
  "v_smfmac_f32_32x32x32_bf16 v[112:127], v[220:223], v[184:191], v30\n" \
  "v_exp_f32 v137, v137\n" \
  "v_exp_f32 v138, v138\n" \
  "v_exp_f32 v139, v139\n" \
  "v_exp_f32 v140, v140\n" \
  "v_exp_f32 v141, v141\n" \
  "v_exp_f32 v142, v142\n" \
  "v_exp_f32 v143, v143\n" \
  "v_mfma_f32_32x32x16_bf16 v[160:175], v[224:227], v[48:51], 0\n" \
  "v_exp_f32 v144, v144\n" \
  "v_exp_f32 v145, v145\n" \
  "v_exp_f32 v146, v146\n" \
  "v_mfma_f32_32x32x16_bf16 v[160:175], v[228:231], v[52:55], v[160:175]\n" \
  "v_exp_f32 v147, v147\n" \
  "v_exp_f32 v148, v148\n" \
  "v_exp_f32 v149, v149\n" \
  "v_smfmac_f32_32x32x32_bf16 v[64:79], v[232:235], v[128:135], v30\n" \
  "v_exp_f32 v150, v150\n" \
  "v_exp_f32 v151, v151\n" \
  "v_exp_f32 v152, v152\n" \
  "v_smfmac_f32_32x32x32_bf16 v[64:79], v[236:239], v[136:143], v30\n" \
  "v_exp_f32 v153, v153\n" \
  "v_exp_f32 v154, v154\n" \
  "v_exp_f32 v155, v155\n" \
  "v_exp_f32 v156, v156\n" \
  "v_exp_f32 v157, v157\n" \
  "v_exp_f32 v158, v158\n" \
  "v_exp_f32 v159, v159\n" \
  "v_mfma_f32_32x32x16_bf16 v[176:191], v[224:227], v[56:59], 0\n" \
  "v_exp_f32 v160, v160\n" \
  "v_exp_f32 v161, v161\n" \
  "v_exp_f32 v162, v162\n" \
  "v_mfma_f32_32x32x16_bf16 v[176:191], v[228:231], v[60:63], v[176:191]\n" \
  "v_exp_f32 v163, v163\n" \
  "v_exp_f32 v164, v164\n" \
  "v_exp_f32 v165, v165\n" \
  "v_smfmac_f32_32x32x32_bf16 v[80:95], v[232:235], v[144:151], v30\n" \
  "v_exp_f32 v166, v166\n" \
  "v_exp_f32 v167, v167\n" \
  "v_exp_f32 v168, v168\n" \
  "v_smfmac_f32_32x32x32_bf16 v[80:95], v[236:239], v[152:159], v30\n" \
  "s_waitcnt vmcnt(0)\n" \
  "v_exp_f32 v169, v169\n" \
  "v_exp_f32 v170, v170\n" \
  "v_exp_f32 v171, v171\n" \
  "v_exp_f32 v172, v172\n" \
  "v_exp_f32 v173, v173\n" \
  "v_exp_f32 v174, v174\n" \
  "v_exp_f32 v175, v175\n" \
  "v_mfma_f32_32x32x16_bf16 v[128:143], v[240:243], v[32:35], 0\n" \
  "v_exp_f32 v176, v176\n" \
  "v_exp_f32 v177, v177\n" \
  "v_exp_f32 v178, v178\n" \
  "v_mfma_f32_32x32x16_bf16 v[128:143], v[244:247], v[36:39], v[128:143]\n" \
  "v_exp_f32 v179, v179\n" \
  "v_exp_f32 v180, v180\n" \
  "v_exp_f32 v181, v181\n" \
  "v_smfmac_f32_32x32x32_bf16 v[96:111], v[232:235], v[160:167], v30\n" \
  "v_exp_f32 v182, v182\n" \
  "v_exp_f32 v183, v183\n" \
  "v_exp_f32 v184, v184\n" \
  "v_smfmac_f32_32x32x32_bf16 v[96:111], v[236:239], v[168:175], v30\n" \
  "v_exp_f32 v185, v185\n" \
  "v_exp_f32 v186, v186\n" \
  "v_exp_f32 v187, v187\n" \
  "v_exp_f32 v188, v188\n" \
  "v_exp_f32 v189, v189\n" \
  "v_exp_f32 v190, v190\n" \
  "v_exp_f32 v191, v191\n" \
    \
  "v_mfma_f32_32x32x16_bf16 v[144:159], v[240:243], v[40:43], 0\n" \
  "v_exp_f32 v128, v128\n" \
  "v_exp_f32 v129, v129\n" \
  "v_exp_f32 v130, v130\n" \
  "v_mfma_f32_32x32x16_bf16 v[144:159], v[244:247], v[44:47], v[144:159]\n" \
  "v_exp_f32 v131, v131\n" \
  "v_exp_f32 v132, v132\n" \
  "v_exp_f32 v133, v133\n" \
  "v_smfmac_f32_32x32x32_bf16 v[112:127], v[232:235], v[176:183], v30\n" \
  "v_exp_f32 v134, v134\n" \
  "v_exp_f32 v135, v135\n" \
  "v_exp_f32 v136, v136\n" \
  "v_smfmac_f32_32x32x32_bf16 v[112:127], v[236:239], v[184:191], v30\n" \
  "v_exp_f32 v137, v137\n" \
  "v_exp_f32 v138, v138\n" \
  "v_exp_f32 v139, v139\n" \
  "v_exp_f32 v140, v140\n" \
  "v_exp_f32 v141, v141\n" \
  "v_exp_f32 v142, v142\n" \
  "v_exp_f32 v143, v143\n" \
  "v_mfma_f32_32x32x16_bf16 v[160:175], v[240:243], v[48:51], 0\n" \
  "v_exp_f32 v144, v144\n" \
  "v_exp_f32 v145, v145\n" \
  "v_exp_f32 v146, v146\n" \
  "v_mfma_f32_32x32x16_bf16 v[160:175], v[244:247], v[52:55], v[160:175]\n" \
  "v_exp_f32 v147, v147\n" \
  "v_exp_f32 v148, v148\n" \
  "v_exp_f32 v149, v149\n" \
  "v_smfmac_f32_32x32x32_bf16 v[64:79], v[248:251], v[128:135], v30\n" \
  "v_exp_f32 v150, v150\n" \
  "v_exp_f32 v151, v151\n" \
  "v_exp_f32 v152, v152\n" \
  "v_smfmac_f32_32x32x32_bf16 v[64:79], v[252:255], v[136:143], v30\n" \
  "v_exp_f32 v153, v153\n" \
  "v_exp_f32 v154, v154\n" \
  "v_exp_f32 v155, v155\n" \
  "v_exp_f32 v156, v156\n" \
  "v_exp_f32 v157, v157\n" \
  "v_exp_f32 v158, v158\n" \
  "v_exp_f32 v159, v159\n" \
  "v_mfma_f32_32x32x16_bf16 v[176:191], v[240:243], v[56:59], 0\n" \
  "v_exp_f32 v160, v160\n" \
  "v_exp_f32 v161, v161\n" \
  "v_exp_f32 v162, v162\n" \
  "v_mfma_f32_32x32x16_bf16 v[176:191], v[244:247], v[60:63], v[176:191]\n" \
  "v_exp_f32 v163, v163\n" \
  "v_exp_f32 v164, v164\n" \
  "v_exp_f32 v165, v165\n" \
  "v_smfmac_f32_32x32x32_bf16 v[80:95], v[248:251], v[144:151], v30\n" \
  "ds_write_b128 v12, v[64:67] offset:0\n" \
  "ds_write_b128 v12, v[68:71] offset:1024\n" \
  "ds_write_b128 v12, v[72:75] offset:2048\n" \
  "ds_write_b128 v12, v[76:79] offset:3072\n" \
  "v_exp_f32 v166, v166\n" \
  "v_exp_f32 v167, v167\n" \
  "v_exp_f32 v168, v168\n" \
  "v_smfmac_f32_32x32x32_bf16 v[80:95], v[252:255], v[152:159], v30\n" \
  "v_exp_f32 v169, v169\n" \
  "v_exp_f32 v170, v170\n" \
  "v_exp_f32 v171, v171\n" \
  "v_exp_f32 v172, v172\n" \
  "v_exp_f32 v173, v173\n" \
  "v_exp_f32 v174, v174\n" \
  "v_exp_f32 v175, v175\n" \
  "v_exp_f32 v176, v176\n" \
  "v_exp_f32 v177, v177\n" \
  "v_exp_f32 v178, v178\n" \
  "v_exp_f32 v179, v179\n" \
  "v_exp_f32 v180, v180\n" \
  "v_exp_f32 v181, v181\n" \
  "v_smfmac_f32_32x32x32_bf16 v[96:111], v[248:251], v[160:167], v30\n" \
  "ds_write_b128 v13, v[80:83] offset:0\n" \
  "ds_write_b128 v13, v[84:87] offset:1024\n" \
  "ds_write_b128 v13, v[88:91] offset:2048\n" \
  "ds_write_b128 v13, v[92:95] offset:3072\n" \
  "v_exp_f32 v182, v182\n" \
  "v_exp_f32 v183, v183\n" \
  "v_exp_f32 v184, v184\n" \
  "v_smfmac_f32_32x32x32_bf16 v[96:111], v[252:255], v[168:175], v30\n" \
  "v_exp_f32 v185, v185\n" \
  "v_exp_f32 v186, v186\n" \
  "v_exp_f32 v187, v187\n" \
  "v_exp_f32 v188, v188\n" \
  "v_exp_f32 v189, v189\n" \
  "v_exp_f32 v190, v190\n" \
  "v_exp_f32 v191, v191\n" \
  "v_smfmac_f32_32x32x32_bf16 v[112:127], v[248:251], v[176:183], v30\n" \
  "s_nop 0\n" \
  "v_smfmac_f32_32x32x32_bf16 v[112:127], v[252:255], v[184:191], v30\n" \
    \
  "v_min_f32 v22, v76, v92\n" \
  "v_max_f32 v23, v76, v92\n" \
  "ds_write_b128 v14, v[96:99] offset:0\n" \
  "ds_write_b128 v14, v[100:103] offset:1024\n" \
  "ds_write_b128 v14, v[104:107] offset:2048\n" \
  "ds_write_b128 v14, v[108:111] offset:3072\n" \
  "s_nop 5\n" \
  "v_min3_f32 v22, v108, v124, v22\n" \
  "v_max3_f32 v23, v108, v124, v23\n" \
  "v_cmp_gt_f32 vcc, 0x21800000, v22\n" \
  "s_mov_b32 s53, vcc_hi\n" \
  "v_cmp_lt_f32 vcc, 0x5c800000, v23\n" \
  "s_or_b32 s53, s53, vcc_hi\n" \
  "s_cselect_b32 s53, 0x101, 1\n" \
  ".Lfz_red:\n" \
  "v_mov_b32 v9, s53\n" \
  "s_mov_b64 exec, 1\n" \
  "ds_add_u32 v8, v9\n" \
  "s_mov_b64 exec, -1\n" \
  "s_mov_b32 s63, 0x1000000\n" \
  ".Lfz_sy0:\n" \
  "ds_read_b32 v9, v8\n" \
  "s_waitcnt lgkmcnt(0)\n" \
  "v_readfirstlane_b32 s54, v9\n" \
  "s_sub_u32 s63, s63, 1\n" \
  "s_cbranch_scc1 .Lfz_sy0x\n" \
  "s_and_b32 s53, s54, 0xff\n" \
  "s_cmp_lt_u32 s53, s60\n" \
  "s_cbranch_scc1 .Lfz_sy0\n" \
  ".Lfz_sy0x:\n" \
  "s_cmp_eq_u32 s60, 4\n" \
  "s_cselect_b32 s54, s54, 0\n" \
  "s_cmp_gt_u32 s54, 0xff\n" \
  "s_cbranch_scc1 .Lfz_slow\n" \
    \
  "ds_read_b128 v[140:143], v15 offset:3072\n" \
  "ds_read_b128 v[156:159], v15 offset:7168\n" \
  "ds_read_b128 v[172:175], v15 offset:11264\n" \
  "ds_read_b128 v[128:131], v15 offset:0\n" \
  "ds_read_b128 v[144:147], v15 offset:4096\n" \
  "ds_read_b128 v[160:163], v15 offset:8192\n" \
  "ds_read_b128 v[132:135], v15 offset:1024\n" \
  "ds_read_b128 v[148:151], v15 offset:5120\n" \
  "ds_read_b128 v[164:167], v15 offset:9216\n" \
  "ds_read_b128 v[136:139], v15 offset:2048\n" \
  "ds_read_b128 v[152:155], v15 offset:6144\n" \
  "ds_read_b128 v[168:171], v15 offset:10240\n" \
  "v_cmp_eq_u32 vcc, 0, v4\n" \
  "s_waitcnt lgkmcnt(9)\n" \
  "v_add_f32 v124, v124, v140\n" \
  "v_add_f32 v156, v156, v172\n" \
  "v_add_f32 v125, v125, v141\n" \
  "v_add_f32 v157, v157, v173\n" \
  "v_add_f32 v126, v126, v142\n" \
  "v_add_f32 v158, v158, v174\n" \
  "v_add_f32 v127, v127, v143\n" \
  "v_add_f32 v159, v159, v175\n" \
  "v_add_f32 v124, v124, v156\n" \
  "v_add_f32 v125, v125, v157\n" \
  "v_add_f32 v126, v126, v158\n" \
  "v_add_f32 v127, v127, v159\n" \
  "v_mov_b32 v22, v124\n" \
  "v_mov_b32 v23, v124\n" \
  "s_waitcnt lgkmcnt(6)\n" \
  "v_add_f32 v112, v112, v128\n" \
  "v_add_f32 v144, v144, v160\n" \
  "v_add_f32 v113, v113, v129\n" \
  "v_add_f32 v145, v145, v161\n" \
  "v_add_f32 v114, v114, v130\n" \
  "v_add_f32 v146, v146, v162\n" \
  "v_add_f32 v115, v115, v131\n" \
  "v_add_f32 v147, v147, v163\n" \
  "v_add_f32 v112, v112, v144\n" \
  "v_add_f32 v113, v113, v145\n" \
  "v_add_f32 v114, v114, v146\n" \
  "v_add_f32 v115, v115, v147\n" \
  "v_permlane32_swap_b32 v22, v23\n" \
  "v_rcp_f32 v22, v23\n" \
  "s_waitcnt lgkmcnt(3)\n" \
  "v_add_f32 v116, v116, v132\n" \
  "v_add_f32 v148, v148, v164\n" \
  "v_add_f32 v117, v117, v133\n" \
  "v_add_f32 v149, v149, v165\n" \
  "v_add_f32 v118, v118, v134\n" \
  "v_add_f32 v150, v150, v166\n" \
  "v_add_f32 v119, v119, v135\n" \
  "v_add_f32 v151, v151, v167\n" \
  "v_add_f32 v116, v116, v148\n" \
  "v_add_f32 v117, v117, v149\n" \
  "v_add_f32 v118, v118, v150\n" \
  "v_add_f32 v119, v119, v151\n" \
  "s_waitcnt lgkmcnt(0)\n" \
  "v_add_f32 v120, v120, v136\n" \
  "v_add_f32 v152, v152, v168\n" \
  "v_add_f32 v121, v121, v137\n" \
  "v_add_f32 v153, v153, v169\n" \
  "v_add_f32 v122, v122, v138\n" \
  "v_add_f32 v154, v154, v170\n" \
  "v_add_f32 v123, v123, v139\n" \
  "v_add_f32 v155, v155, v171\n" \
  "v_add_f32 v120, v120, v152\n" \
  "v_add_f32 v121, v121, v153\n" \
  "v_add_f32 v122, v122, v154\n" \
  "v_add_f32 v123, v123, v155\n" \
  "v_mul_f32 v112, v22, v112\n" \
  "v_mul_f32 v113, v22, v113\n" \
  "v_mul_f32 v114, v22, v114\n" \
  "v_mul_f32 v115, v22, v115\n" \
  "v_mul_f32 v116, v22, v116\n" \
  "v_mul_f32 v117, v22, v117\n" \
  "v_mul_f32 v118, v22, v118\n" \
  "v_mul_f32 v119, v22, v119\n" \
  "v_cvt_pk_bf16_f32 v32, v112, v113\n" \
  "v_cvt_pk_bf16_f32 v33, v114, v115\n" \
  "v_cvt_pk_bf16_f32 v34, v116, v117\n" \
  "v_cvt_pk_bf16_f32 v35, v118, v119\n" \
  "v_min_u32 v32, -2, v32\n" \
  "v_min_u32 v33, -2, v33\n" \
  "v_min_u32 v34, -2, v34\n" \
  "v_min_u32 v35, -2, v35\n" \
  "buffer_store_dwordx4 v[32:35], v2, s[36:39], s34 offen offset:0 sc1\n" \
  "v_mul_f32 v120, v22, v120\n" \
  "v_mul_f32 v121, v22, v121\n" \
  "v_mul_f32 v122, v22, v122\n" \
  "v_mul_f32 v123, v22, v123\n" \
  "v_mul_f32 v124, v22, v124\n" \
  "v_mul_f32 v125, v22, v125\n" \
  "v_mul_f32 v126, v22, v126\n" \
  "v_mul_f32 v127, v22, v127\n" \
  "v_cvt_pk_bf16_f32 v36, v120, v121\n" \
  "v_cvt_pk_bf16_f32 v37, v122, v123\n" \
  "v_cvt_pk_bf16_f32 v38, v124, v125\n" \
  "v_cvt_pk_bf16_f32 v39, v126, v127\n" \
  "v_min_u32 v36, -2, v36\n" \
  "v_min_u32 v37, -2, v37\n" \
  "v_min_u32 v38, -2, v38\n" \
  "v_min_u32 v39, -2, v39\n" \
  "v_cndmask_b32 v38, 0, v38, vcc\n" \
  "v_cndmask_b32 v39, 0, v39, vcc\n" \
  "buffer_store_dwordx4 v[36:39], v2, s[36:39], s34 offen offset:1024 sc1\n" \
    \
  "s_setprio 0\n" \
  "s_cmp_lt_u32 s2, 32\n" \
  "s_cbranch_scc1 .Lfz_A_sp\n" \
  "s_lshl1_add_u32 s61, s52, 1\n" \
  "s_mul_i32 s61, s61, 28672\n" \
  "s_lshl_b32 s53, s48, 7\n" \
  "s_add_u32 s61, s61, s53\n" \
  "v_bfe_u32 v12, v0, 3, 5\n" \
  "v_mul_u32_u24 v12, 896, v12\n" \
  "v_and_b32 v13, 7, v0\n" \
  "v_lshl_add_u32 v12, v13, 4, v12\n" \
  "buffer_load_dwordx4 v[16:19], v12, s[4:7], s61 offen\n" \
  "s_barrier\n" \
  "v_bfe_u32 v14, v0, 3, 3\n" \
  "v_bfe_u32 v15, v0, 4, 1\n" \
  "v_lshl_or_b32 v14, v15, 3, v14\n" \
  "v_and_b32 v15, 7, v0\n" \
  "v_xor_b32 v14, v14, v15\n" \
  "v_bfe_u32 v15, v0, 3, 5\n" \
  "v_lshlrev_b32 v15, 8, v15\n" \
  "v_lshl_or_b32 v14, v14, 4, v15\n" \
  "v_xor_b32 v15, 0x80, v14\n" \
  "s_barrier\n" \
  "s_waitcnt vmcnt(0)\n" \
  "v_lshlrev_b32 v40, 16, v16\n" \
  "v_and_b32 v41, 0xffff0000, v16\n" \
  "v_lshlrev_b32 v42, 16, v17\n" \
  "v_and_b32 v43, 0xffff0000, v17\n" \
  "v_lshlrev_b32 v44, 16, v18\n" \
  "v_and_b32 v45, 0xffff0000, v18\n" \
  "v_lshlrev_b32 v46, 16, v19\n" \
  "v_and_b32 v47, 0xffff0000, v19\n" \
  "s_barrier\n" \
  "ds_read_b128 v[48:51], v14 offset:40960\n" \
  "ds_read_b128 v[52:55], v15 offset:40960\n" \
  "ds_read_b128 v[56:59], v14 offset:57344\n" \
  "ds_read_b128 v[60:63], v15 offset:57344\n" \
  "s_waitcnt lgkmcnt(2)\n" \
  "v_add_f32 v40, v40, v48\n" \
  "v_add_f32 v41, v41, v49\n" \
  "v_add_f32 v42, v42, v50\n" \
  "v_add_f32 v43, v43, v51\n" \
  "v_add_f32 v44, v44, v52\n" \
  "v_add_f32 v45, v45, v53\n" \
  "v_add_f32 v46, v46, v54\n" \
  "v_add_f32 v47, v47, v55\n" \
  "s_waitcnt lgkmcnt(0)\n" \
  "v_add_f32 v40, v40, v56\n" \
  "v_add_f32 v41, v41, v57\n" \
  "v_add_f32 v42, v42, v58\n" \
  "v_add_f32 v43, v43, v59\n" \
  "v_add_f32 v44, v44, v60\n" \
  "v_add_f32 v45, v45, v61\n" \
  "v_add_f32 v46, v46, v62\n" \
  "v_add_f32 v47, v47, v63\n" \
  "v_cvt_pk_bf16_f32 v48, v40, v41\n" \
  "v_cvt_pk_bf16_f32 v49, v42, v43\n" \
  "v_cvt_pk_bf16_f32 v50, v44, v45\n" \
  "v_cvt_pk_bf16_f32 v51, v46, v47\n" \
  "buffer_store_dwordx4 v[48:51], v12, s[40:43], s61 offen sc0 sc1\n" \
  ".Lfz_A_end:\n" \
  "s_endpgm\n" \
  ".Lfz_A_sp:\n" \
  "s_or_b32 s53, s2, s45\n" \
  "s_cmp_lg_u32 s53, 0\n" \
  "s_cbranch_scc1 .Lfz_A_end\n" \
    \
  "v_bfe_u32 v16, v0, 4, 2\n" \
  "v_lshlrev_b32 v16, 11, v16\n" \
  "v_and_b32 v17, 15, v0\n" \
  "v_min_u32 v17, 13, v17\n" \
  "v_lshl_add_u32 v16, v17, 7, v16\n" \
  "v_add_u32 v16, 9949184, v16\n" \
  "s_mov_b32 s54, 4\n" \
  ".Lfz_ep_p:\n" \
  "s_mov_b32 s63, 0x100000\n" \
  ".Lfz_poll0:\n" \
  "buffer_load_dword v17, v16, s[36:39], 0 offen sc1\n" \
  "s_waitcnt vmcnt(0)\n" \
  "v_cmp_ne_u32 vcc, s51, v17\n" \
  "s_sub_u32 s63, s63, 1\n" \
  "s_cbranch_vccz .Lfz_poll0x\n" \
  "s_sleep 32\n" \
  "s_cmp_lg_u32 s63, 0\n" \
  "s_cbranch_scc1 .Lfz_poll0\n" \
  ".Lfz_poll0x:\n" \
  "v_add_u32 v16, 8192, v16\n" \
  "s_sub_u32 s54, s54, 1\n" \
  "s_cmp_lg_u32 s54, 0\n" \
  "s_cbranch_scc1 .Lfz_ep_p\n" \
  "v_mov_b32 v16, 9981952\n" \
  "v_mov_b32 v17, s51\n" \
  "s_mov_b64 exec, 1\n" \
  "buffer_store_dword v17, v16, s[36:39], 0 offen sc1\n" \
  "s_branch .Lfz_A_end\n" \
  ".Lfz_G:\n" \
    \
  "v_and_b32 v1, 63, v0\n" \
  "v_lshlrev_b32 v2, 4, v1\n" \
  "v_and_b32 v3, 31, v0\n" \
  "v_bfe_u32 v4, v0, 5, 1\n" \
  "v_bfe_u32 v5, v0, 2, 2\n" \
  "v_bfe_u32 v7, v0, 4, 1\n" \
  "v_and_b32 v6, 3, v0\n" \
  "v_lshl_add_u32 v6, v7, 2, v6\n" \
  "v_bfe_u32 v7, v0, 1, 3\n" \
  "v_xor_b32 v7, v7, v4\n" \
  "v_lshlrev_b32 v7, 4, v7\n" \
  "v_lshl_add_u32 v7, v3, 7, v7\n" \
  "v_mov_b64 v[8:9], 0\n" \
  "v_mov_b32 v10, 159744\n" \
  "ds_write_b64 v10, v[8:9]\n" \
  "v_bfe_u32 v21, v0, 4, 2\n" \
  "v_lshl_add_u32 v22, v21, 3, v5\n" \
  "v_lshlrev_b32 v22, 8, v22\n" \
  "v_xor_b32 v23, s45, v5\n" \
  "v_lshl_add_u32 v22, v23, 6, v22\n" \
  "v_lshl_add_u32 v16, v6, 3, v22\n" \
  "v_xor_b32 v17, 32, v16\n" \
  "v_and_b32 v23, 15, v0\n" \
  "v_bfe_u32 v22, v0, 1, 3\n" \
  "v_xor_b32 v22, v22, v21\n" \
  "v_lshlrev_b32 v22, 4, v22\n" \
  "v_lshl_add_u32 v18, v23, 7, v22\n" \
  "v_xor_b32 v19, 64, v18\n" \
  "v_lshlrev_b32 v23, 4, v23\n" \
  "v_lshl_add_u32 v21, v21, 9, v23\n" \
  "v_add_u32 v23, 57344, v23\n" \
  "s_lshl_b32 s53, s45, 10\n" \
  "s_add_u32 s53, s53, 53248\n" \
  "v_add_u32 v22, s53, v2\n" \
  "s_waitcnt lgkmcnt(0)\n" \
  "s_setprio 3\n" \
  "v_mov_b32 v20, 9981952\n" \
  "buffer_load_dword v20, v20, s[36:39], 0 offen sc1\n" \
  "s_setprio 0\n" \
  "v_bfe_u32 v8, v0, 3, 3\n" \
  "v_bfe_u32 v9, v0, 4, 1\n" \
  "v_lshlrev_b32 v9, 2, v9\n" \
  "v_and_b32 v30, 7, v0\n" \
  "v_xor_b32 v9, v9, v30\n" \
  "v_mul_u32_u24 v8, 896, v8\n" \
  "v_lshl_add_u32 v8, v9, 4, v8\n" \
  "v_add_u32 v9, 6144, v8\n" \
  "s_lshl_b32 s59, s45, 11\n" \
  "s_add_u32 s59, s59, 69632\n" \
  "v_lshl_add_u32 v10, v4, 2, v5\n" \
  "v_lshlrev_b32 v10, 7, v10\n" \
  "v_lshl_add_u32 v10, v6, 3, v10\n" \
  "v_bfe_u32 v29, v0, 3, 1\n" \
  "v_lshl_add_u32 v10, v29, 6, v10\n" \
  "v_add_u32 v10, s59, v10\n" \
  "v_xor_b32 v11, 64, v10\n" \
  "v_add_u32 v12, 57344, v10\n" \
  "v_add_u32 v13, 57344, v11\n" \
  "s_lshr_b32 s53, s52, 1\n" \
  "s_mul_i32 s54, s45, 7\n" \
  "s_lshr_b32 s54, s54, 1\n" \
  "s_lshl4_add_u32 s53, s53, s54\n" \
  "s_lshl_b32 s53, s53, 7\n" \
  "v_and_b32 v29, 3, v0\n" \
  "s_add_u32 s54, s53, 9949184\n" \
  "v_lshl_add_u32 v26, v29, 7, s54\n" \
  "s_and_b32 s54, s45, 1\n" \
  "s_mul_i32 s53, s54, 147456\n" \
  "v_add_u32 v24, s53, v2\n" \
  "s_sub_u32 s53, 147456, s53\n" \
  "v_add_u32 v25, s53, v2\n" \
  "s_lshl1_add_u32 s53, s52, s54\n" \
  "s_lshl_b32 s53, s53, 8\n" \
  "s_add_u32 s53, s53, 9699328\n" \
  "v_lshl_add_u32 v27, v3, 3, s53\n" \
  "v_bfe_u32 v29, v0, 3, 5\n" \
  "v_mul_u32_u24 v29, 896, v29\n" \
  "v_lshl_add_u32 v14, v30, 4, v29\n" \
  "v_add_u32 v15, 13312, v8\n" \
  "v_mul_u32_u24 v7, 3584, v4\n" \
  "v_sub_u32 v7, v9, v7\n" \
  "s_lshl_b32 s60, s48, 7\n" \
  "s_mul_i32 s61, s52, 57344\n" \
  "s_add_u32 s61, s61, s60\n" \
  "s_mul_i32 s55, s45, 401408\n" \
  "s_add_u32 s55, s55, s60\n" \
  "s_mul_i32 s56, s45, 100352\n" \
  "s_add_u32 s56, s56, s60\n" \
  "s_mul_i32 s57, s52, 288\n" \
  "s_lshr_b32 s53, s45, 1\n" \
  "s_lshl4_add_u32 s58, s53, s57\n" \
  "s_add_u32 s58, s58, 112\n" \
  "s_lshl_b32 s58, s58, 10\n" \
  "s_mul_i32 s53, s45, 28\n" \
  "s_add_u32 s57, s57, s53\n" \
  "s_lshl_b32 s57, s57, 10\n" \
  "s_lshl4_add_u32 s60, s46, s50\n" \
  "s_lshl_b32 s60, s60, 7\n" \
  "s_add_u32 s60, s60, 9949184\n" \
  "s_mul_i32 s86, s46, 576\n" \
  "s_lshl2_add_u32 s54, s50, s45\n" \
  "s_lshl1_add_u32 s86, s54, s86\n" \
  "s_lshl_b32 s86, s86, 10\n" \
  "s_bfe_u32 s54, s2, 0x20003\n" \
  "s_lshl1_add_u32 s94, s54, s48\n" \
  "s_sub_u32 s54, s94, 7\n" \
  "s_cmp_ge_u32 s94, 7\n" \
  "s_cselect_b32 s94, s54, s94\n" \
  "s_mul_i32 s54, s94, 57344\n" \
  "s_add_u32 s55, s55, s54\n" \
  "s_lshl_b32 s54, s94, 12\n" \
  "s_add_u32 s57, s57, s54\n" \
  "s_mov_b32 s95, s94\n" \
  "s_lshl2_add_u32 s92, s46, s45\n" \
  "s_mul_i32 s92, s92, 144\n" \
  "s_lshl1_add_u32 s92, s49, s92\n" \
  "s_add_u32 s92, s92, 112\n" \
  "s_lshl_b32 s92, s92, 10\n" \
  "s_add_u32 s93, s45, 3\n" \
  "v_mov_b64 v[28:29], -1\n" \
  "v_mov_b64 v[30:31], -1\n" \
  "v_mov_b64 v[128:129], 0\n" \
  "v_mov_b64 v[130:131], 0\n" \
  "v_mov_b64 v[132:133], 0\n" \
  "v_mov_b64 v[134:135], 0\n" \
  "v_mov_b64 v[136:137], 0\n" \
  "v_mov_b64 v[138:139], 0\n" \
  "v_mov_b64 v[140:141], 0\n" \
  "v_mov_b64 v[142:143], 0\n" \
  "v_mov_b64 v[144:145], 0\n" \
  "v_mov_b64 v[146:147], 0\n" \
  "v_mov_b64 v[148:149], 0\n" \
  "v_mov_b64 v[150:151], 0\n" \
  "v_mov_b64 v[152:153], 0\n" \
  "v_mov_b64 v[154:155], 0\n" \
  "v_mov_b64 v[156:157], 0\n" \
  "v_mov_b64 v[158:159], 0\n" \
  "v_mov_b64 v[160:161], 0\n" \
  "v_mov_b64 v[162:163], 0\n" \
  "v_mov_b64 v[164:165], 0\n" \
  "v_mov_b64 v[166:167], 0\n" \
  "v_mov_b64 v[168:169], 0\n" \
  "v_mov_b64 v[170:171], 0\n" \
  "v_mov_b64 v[172:173], 0\n" \
  "v_mov_b64 v[174:175], 0\n" \
  "v_mov_b64 v[176:177], 0\n" \
  "v_mov_b64 v[178:179], 0\n" \
  "v_mov_b64 v[180:181], 0\n" \
  "v_mov_b64 v[182:183], 0\n" \
  "v_mov_b64 v[184:185], 0\n" \
  "v_mov_b64 v[186:187], 0\n" \
  "v_mov_b64 v[188:189], 0\n" \
  "v_mov_b64 v[190:191], 0\n" \
  "v_mov_b64 v[192:193], 0\n" \
  "v_mov_b64 v[194:195], 0\n" \
  "v_mov_b64 v[196:197], 0\n" \
  "v_mov_b64 v[198:199], 0\n" \
  "v_mov_b64 v[200:201], 0\n" \
  "v_mov_b64 v[202:203], 0\n" \
  "v_mov_b64 v[204:205], 0\n" \
  "v_mov_b64 v[206:207], 0\n" \
  "v_mov_b64 v[208:209], 0\n" \
  "v_mov_b64 v[210:211], 0\n" \
  "v_mov_b64 v[212:213], 0\n" \
  "v_mov_b64 v[214:215], 0\n" \
  "v_mov_b64 v[216:217], 0\n" \
  "v_mov_b64 v[218:219], 0\n" \
  "v_mov_b64 v[220:221], 0\n" \
  "v_mov_b64 v[222:223], 0\n" \
  "v_mov_b64 v[224:225], 0\n" \
  "v_mov_b64 v[226:227], 0\n" \
  "v_mov_b64 v[228:229], 0\n" \
  "v_mov_b64 v[230:231], 0\n" \
  "v_mov_b64 v[232:233], 0\n" \
  "v_mov_b64 v[234:235], 0\n" \
  "v_mov_b64 v[236:237], 0\n" \
  "v_mov_b64 v[238:239], 0\n" \
  "v_mov_b64 v[240:241], 0\n" \
  "v_mov_b64 v[242:243], 0\n" \
  "v_mov_b64 v[244:245], 0\n" \
  "s_waitcnt vmcnt(1)\n" \
  "v_mov_b64 v[246:247], 0\n" \
  "v_mov_b64 v[248:249], 0\n" \
  "v_mov_b64 v[250:251], 0\n" \
  "v_mov_b64 v[252:253], 0\n" \
  "v_mov_b64 v[254:255], 0\n" \
  "s_mov_b32 s84, 6\n" \
  "s_mov_b32 s85, 0\n" \
  "s_setprio 2\n" \
  "s_barrier\n" \
  "ds_read_b64_tr_b16 v[32:33], v16 offset:16384\n" \
  "ds_read_b64_tr_b16 v[34:35], v16 offset:17408\n" \
  "ds_read_b128 v[48:51], v18 offset:0\n" \
  "ds_read_b64_tr_b16 v[36:37], v17 offset:16384\n" \
  "ds_read_b64_tr_b16 v[38:39], v17 offset:17408\n" \
  "ds_read_b64_tr_b16 v[40:41], v16 offset:32768\n" \
  "ds_read_b64_tr_b16 v[42:43], v16 offset:33792\n" \
  "ds_read_b64_tr_b16 v[44:45], v17 offset:32768\n" \
  "ds_read_b64_tr_b16 v[46:47], v17 offset:33792\n" \
  "ds_read_b128 v[52:55], v18 offset:2048\n" \
  "ds_read_b128 v[56:59], v18 offset:4096\n" \
  "ds_read_b128 v[60:63], v18 offset:6144\n" \
  "ds_read_b128 v[64:67], v18 offset:8192\n" \
  "ds_read_b128 v[68:71], v18 offset:10240\n" \
  "ds_read_b128 v[72:75], v18 offset:12288\n" \
  "ds_read_b128 v[76:79], v18 offset:14336\n" \
  "s_waitcnt lgkmcnt(7)\n" \
  ".Lfz_g1:\n" \
  "v_mfma_f32_16x16x32_bf16 v[128:131], v[32:35], v[48:51], v[128:131]\n" \
  "ds_read_b64_tr_b16 v[80:81], v16 offset:24576\n" \
  "v_mfma_f32_16x16x32_bf16 v[132:135], v[36:39], v[48:51], v[132:135]\n" \
  "ds_read_b64_tr_b16 v[82:83], v16 offset:25600\n" \
  "v_mfma_f32_16x16x32_bf16 v[136:139], v[40:43], v[48:51], v[136:139]\n" \
  "ds_read_b128 v[96:99], v19 offset:0\n" \
  "v_mfma_f32_16x16x32_bf16 v[140:143], v[44:47], v[48:51], v[140:143]\n" \
  "ds_read_b64_tr_b16 v[84:85], v17 offset:24576\n" \
  "s_waitcnt lgkmcnt(10)\n" \
  "v_mfma_f32_16x16x32_bf16 v[156:159], v[44:47], v[52:55], v[156:159]\n" \
  "ds_read_b64_tr_b16 v[86:87], v17 offset:25600\n" \
  "v_mfma_f32_16x16x32_bf16 v[152:155], v[40:43], v[52:55], v[152:155]\n" \
  "ds_read_b64_tr_b16 v[88:89], v16 offset:40960\n" \
  "v_mfma_f32_16x16x32_bf16 v[148:151], v[36:39], v[52:55], v[148:151]\n" \
  "ds_read_b64_tr_b16 v[90:91], v16 offset:41984\n" \
  "v_mfma_f32_16x16x32_bf16 v[144:147], v[32:35], v[52:55], v[144:147]\n" \
  "ds_read_b64_tr_b16 v[92:93], v17 offset:40960\n" \
  "s_waitcnt lgkmcnt(13)\n" \
  "v_mfma_f32_16x16x32_bf16 v[160:163], v[32:35], v[56:59], v[160:163]\n" \
  "ds_read_b64_tr_b16 v[94:95], v17 offset:41984\n" \
  "v_mfma_f32_16x16x32_bf16 v[164:167], v[36:39], v[56:59], v[164:167]\n" \
  "ds_read_b128 v[100:103], v19 offset:2048\n" \
  "v_mfma_f32_16x16x32_bf16 v[168:171], v[40:43], v[56:59], v[168:171]\n" \
  "ds_read_b128 v[104:107], v19 offset:4096\n" \
  "v_mfma_f32_16x16x32_bf16 v[172:175], v[44:47], v[56:59], v[172:175]\n" \
  "ds_read_b128 v[108:111], v19 offset:6144\n" \
  "s_waitcnt lgkmcnt(14)\n" \
  "v_mfma_f32_16x16x32_bf16 v[188:191], v[44:47], v[60:63], v[188:191]\n" \
  "ds_read_b128 v[112:115], v19 offset:8192\n" \
  "v_mfma_f32_16x16x32_bf16 v[184:187], v[40:43], v[60:63], v[184:187]\n" \
  "ds_read_b128 v[116:119], v19 offset:10240\n" \
  "v_mfma_f32_16x16x32_bf16 v[180:183], v[36:39], v[60:63], v[180:183]\n" \
  "ds_read_b128 v[120:123], v19 offset:12288\n" \
  "v_mfma_f32_16x16x32_bf16 v[176:179], v[32:35], v[60:63], v[176:179]\n" \
  "ds_read_b128 v[124:127], v19 offset:14336\n" \
  "s_waitcnt lgkmcnt(14)\n" \
  "v_mfma_f32_16x16x32_bf16 v[192:195], v[32:35], v[64:67], v[192:195]\n" \
  "v_mfma_f32_16x16x32_bf16 v[196:199], v[36:39], v[64:67], v[196:199]\n" \
  "s_mov_b32 s53, 53248\n" \
  "v_mfma_f32_16x16x32_bf16 v[200:203], v[40:43], v[64:67], v[200:203]\n" \
  "s_cmp_eq_u32 s85, 106496\n" \
  "v_mfma_f32_16x16x32_bf16 v[204:207], v[44:47], v[64:67], v[204:207]\n" \
  "s_cselect_b32 s53, 4294860800, s53\n" \
  "s_waitcnt lgkmcnt(14)\n" \
  "v_mfma_f32_16x16x32_bf16 v[220:223], v[44:47], v[68:71], v[220:223]\n" \
  "s_add_u32 s85, s85, s53\n" \
  "v_mfma_f32_16x16x32_bf16 v[216:219], v[40:43], v[68:71], v[216:219]\n" \
  "v_add_u32 v16, s53, v16\n" \
  "v_mfma_f32_16x16x32_bf16 v[212:215], v[36:39], v[68:71], v[212:215]\n" \
  "v_add_u32 v17, s53, v17\n" \
  "v_mfma_f32_16x16x32_bf16 v[208:211], v[32:35], v[68:71], v[208:211]\n" \
  "v_add_u32 v18, s53, v18\n" \
  "s_waitcnt lgkmcnt(14)\n" \
  "v_mfma_f32_16x16x32_bf16 v[224:227], v[32:35], v[72:75], v[224:227]\n" \
  "v_add_u32 v19, s53, v19\n" \
  "v_mfma_f32_16x16x32_bf16 v[228:231], v[36:39], v[72:75], v[228:231]\n" \
  "s_cmp_lg_u32 s84, s93\n" \
  "v_mfma_f32_16x16x32_bf16 v[232:235], v[40:43], v[72:75], v[232:235]\n" \
  "s_cbranch_scc1 .Lfz_noclr\n" \
  "buffer_store_dwordx4 v[28:31], v2, s[36:39], s92 offen sc1\n" \
  "buffer_store_dwordx4 v[28:31], v2, s[36:39], s92 offen offset:1024 sc1\n" \
  ".Lfz_noclr:\n" \
  "v_mfma_f32_16x16x32_bf16 v[236:239], v[44:47], v[72:75], v[236:239]\n" \
  "s_waitcnt lgkmcnt(14)\n" \
  "v_mfma_f32_16x16x32_bf16 v[252:255], v[44:47], v[76:79], v[252:255]\n" \
  "v_mfma_f32_16x16x32_bf16 v[248:251], v[40:43], v[76:79], v[248:251]\n" \
  "s_cmp_eq_u32 s84, 0\n" \
  "v_mfma_f32_16x16x32_bf16 v[244:247], v[36:39], v[76:79], v[244:247]\n" \
  "s_waitcnt lgkmcnt(0)\n" \
  "v_mfma_f32_16x16x32_bf16 v[240:243], v[32:35], v[76:79], v[240:243]\n" \
  "s_cbranch_scc1 .Lfz_g1x\n" \
  "v_mfma_f32_16x16x32_bf16 v[128:131], v[80:83], v[96:99], v[128:131]\n" \
  "s_barrier\n" \
  "v_mfma_f32_16x16x32_bf16 v[132:135], v[84:87], v[96:99], v[132:135]\n" \
  "ds_read_b64_tr_b16 v[32:33], v16 offset:16384\n" \
  "v_mfma_f32_16x16x32_bf16 v[136:139], v[88:91], v[96:99], v[136:139]\n" \
  "ds_read_b64_tr_b16 v[34:35], v16 offset:17408\n" \
  "v_mfma_f32_16x16x32_bf16 v[140:143], v[92:95], v[96:99], v[140:143]\n" \
  "ds_read_b128 v[48:51], v18 offset:0\n" \
  "v_mfma_f32_16x16x32_bf16 v[156:159], v[92:95], v[100:103], v[156:159]\n" \
  "ds_read_b64_tr_b16 v[36:37], v17 offset:16384\n" \
  "v_mfma_f32_16x16x32_bf16 v[152:155], v[88:91], v[100:103], v[152:155]\n" \
  "ds_read_b64_tr_b16 v[38:39], v17 offset:17408\n" \
  "v_mfma_f32_16x16x32_bf16 v[148:151], v[84:87], v[100:103], v[148:151]\n" \
  "ds_read_b64_tr_b16 v[40:41], v16 offset:32768\n" \
  "v_mfma_f32_16x16x32_bf16 v[144:147], v[80:83], v[100:103], v[144:147]\n" \
  "ds_read_b64_tr_b16 v[42:43], v16 offset:33792\n" \
  "v_mfma_f32_16x16x32_bf16 v[160:163], v[80:83], v[104:107], v[160:163]\n" \
  "ds_read_b64_tr_b16 v[44:45], v17 offset:32768\n" \
  "v_mfma_f32_16x16x32_bf16 v[164:167], v[84:87], v[104:107], v[164:167]\n" \
  "ds_read_b64_tr_b16 v[46:47], v17 offset:33792\n" \
  "v_mfma_f32_16x16x32_bf16 v[168:171], v[88:91], v[104:107], v[168:171]\n" \
  "ds_read_b128 v[52:55], v18 offset:2048\n" \
  "v_mfma_f32_16x16x32_bf16 v[172:175], v[92:95], v[104:107], v[172:175]\n" \
  "ds_read_b128 v[56:59], v18 offset:4096\n" \
  "v_mfma_f32_16x16x32_bf16 v[188:191], v[92:95], v[108:111], v[188:191]\n" \
  "ds_read_b128 v[60:63], v18 offset:6144\n" \
  "v_mfma_f32_16x16x32_bf16 v[184:187], v[88:91], v[108:111], v[184:187]\n" \
  "ds_read_b128 v[64:67], v18 offset:8192\n" \
  "v_mfma_f32_16x16x32_bf16 v[180:183], v[84:87], v[108:111], v[180:183]\n" \
  "ds_read_b128 v[68:71], v18 offset:10240\n" \
  "v_mfma_f32_16x16x32_bf16 v[176:179], v[80:83], v[108:111], v[176:179]\n" \
  "ds_read_b128 v[72:75], v18 offset:12288\n" \
  "v_mfma_f32_16x16x32_bf16 v[192:195], v[80:83], v[112:115], v[192:195]\n" \
  "ds_read_b128 v[76:79], v18 offset:14336\n" \
  "v_mfma_f32_16x16x32_bf16 v[196:199], v[84:87], v[112:115], v[196:199]\n" \
  "v_mfma_f32_16x16x32_bf16 v[200:203], v[88:91], v[112:115], v[200:203]\n" \
  "v_mfma_f32_16x16x32_bf16 v[204:207], v[92:95], v[112:115], v[204:207]\n" \
  "v_mfma_f32_16x16x32_bf16 v[220:223], v[92:95], v[116:119], v[220:223]\n" \
  "v_mfma_f32_16x16x32_bf16 v[216:219], v[88:91], v[116:119], v[216:219]\n" \
  "s_sub_u32 s84, s84, 1\n" \
  "v_mfma_f32_16x16x32_bf16 v[212:215], v[84:87], v[116:119], v[212:215]\n" \
  "v_mfma_f32_16x16x32_bf16 v[208:211], v[80:83], v[116:119], v[208:211]\n" \
  "v_mfma_f32_16x16x32_bf16 v[224:227], v[80:83], v[120:123], v[224:227]\n" \
  "v_mfma_f32_16x16x32_bf16 v[228:231], v[84:87], v[120:123], v[228:231]\n" \
  "v_mfma_f32_16x16x32_bf16 v[232:235], v[88:91], v[120:123], v[232:235]\n" \
  "v_mfma_f32_16x16x32_bf16 v[236:239], v[92:95], v[120:123], v[236:239]\n" \
  "v_mfma_f32_16x16x32_bf16 v[252:255], v[92:95], v[124:127], v[252:255]\n" \
  "v_mfma_f32_16x16x32_bf16 v[248:251], v[88:91], v[124:127], v[248:251]\n" \
  "v_mfma_f32_16x16x32_bf16 v[244:247], v[84:87], v[124:127], v[244:247]\n" \
  "v_mfma_f32_16x16x32_bf16 v[240:243], v[80:83], v[124:127], v[240:243]\n" \
  "s_waitcnt lgkmcnt(7)\n" \
  "s_branch .Lfz_g1\n" \
  ".Lfz_g1x:\n" \
  "v_mfma_f32_16x16x32_bf16 v[128:131], v[80:83], v[96:99], v[128:131]\n" \
  "v_mfma_f32_16x16x32_bf16 v[132:135], v[84:87], v[96:99], v[132:135]\n" \
  "v_mfma_f32_16x16x32_bf16 v[136:139], v[88:91], v[96:99], v[136:139]\n" \
  "s_waitcnt vmcnt(0)\n" \
  "v_mfma_f32_16x16x32_bf16 v[140:143], v[92:95], v[96:99], v[140:143]\n" \
  "v_readfirstlane_b32 s51, v20\n" \
  "s_nop 0\n" \
  "s_add_u32 s51, s51, 1\n" \
  "v_mfma_f32_16x16x32_bf16 v[156:159], v[92:95], v[100:103], v[156:159]\n" \
  "v_mfma_f32_16x16x32_bf16 v[152:155], v[88:91], v[100:103], v[152:155]\n" \
  "v_mfma_f32_16x16x32_bf16 v[148:151], v[84:87], v[100:103], v[148:151]\n" \
  "v_mfma_f32_16x16x32_bf16 v[144:147], v[80:83], v[100:103], v[144:147]\n" \
  "v_mfma_f32_16x16x32_bf16 v[160:163], v[80:83], v[104:107], v[160:163]\n" \
  "v_mfma_f32_16x16x32_bf16 v[164:167], v[84:87], v[104:107], v[164:167]\n" \
  "v_mfma_f32_16x16x32_bf16 v[168:171], v[88:91], v[104:107], v[168:171]\n" \
  "v_mfma_f32_16x16x32_bf16 v[172:175], v[92:95], v[104:107], v[172:175]\n" \
  "v_mfma_f32_16x16x32_bf16 v[188:191], v[92:95], v[108:111], v[188:191]\n" \
  "v_mfma_f32_16x16x32_bf16 v[184:187], v[88:91], v[108:111], v[184:187]\n" \
  "v_mfma_f32_16x16x32_bf16 v[180:183], v[84:87], v[108:111], v[180:183]\n" \
  "v_mfma_f32_16x16x32_bf16 v[176:179], v[80:83], v[108:111], v[176:179]\n" \
  "v_mfma_f32_16x16x32_bf16 v[192:195], v[80:83], v[112:115], v[192:195]\n" \
  "v_mfma_f32_16x16x32_bf16 v[196:199], v[84:87], v[112:115], v[196:199]\n" \
  "v_mfma_f32_16x16x32_bf16 v[200:203], v[88:91], v[112:115], v[200:203]\n" \
  "v_mfma_f32_16x16x32_bf16 v[204:207], v[92:95], v[112:115], v[204:207]\n" \
  "v_mfma_f32_16x16x32_bf16 v[220:223], v[92:95], v[116:119], v[220:223]\n" \
  "v_mfma_f32_16x16x32_bf16 v[216:219], v[88:91], v[116:119], v[216:219]\n" \
  "s_barrier\n" \
  "v_mfma_f32_16x16x32_bf16 v[212:215], v[84:87], v[116:119], v[212:215]\n" \
  "ds_read_b64 v[32:33], v22\n" \
  "v_mfma_f32_16x16x32_bf16 v[208:211], v[80:83], v[116:119], v[208:211]\n" \
  "ds_read_b64 v[34:35], v23 offset:0\n" \
  "v_mfma_f32_16x16x32_bf16 v[224:227], v[80:83], v[120:123], v[224:227]\n" \
  "ds_read_b64 v[36:37], v23 offset:256\n" \
  "v_mfma_f32_16x16x32_bf16 v[228:231], v[84:87], v[120:123], v[228:231]\n" \
  "ds_read_b64 v[38:39], v23 offset:1024\n" \
  "v_mfma_f32_16x16x32_bf16 v[232:235], v[88:91], v[120:123], v[232:235]\n" \
  "ds_read_b64 v[40:41], v23 offset:1280\n" \
  "v_mfma_f32_16x16x32_bf16 v[236:239], v[92:95], v[120:123], v[236:239]\n" \
  "ds_read_b64 v[42:43], v23 offset:2048\n" \
  "v_mfma_f32_16x16x32_bf16 v[252:255], v[92:95], v[124:127], v[252:255]\n" \
  "ds_read_b64 v[44:45], v23 offset:2304\n" \
  "v_mfma_f32_16x16x32_bf16 v[248:251], v[88:91], v[124:127], v[248:251]\n" \
  "ds_read_b64 v[46:47], v23 offset:3072\n" \
  "v_mfma_f32_16x16x32_bf16 v[244:247], v[84:87], v[124:127], v[244:247]\n" \
  "ds_read_b64 v[48:49], v23 offset:3328\n" \
  "v_mfma_f32_16x16x32_bf16 v[240:243], v[80:83], v[124:127], v[240:243]\n" \
    \
  "s_waitcnt lgkmcnt(7)\n" \
  "v_mfma_f32_16x16x4_4b_bf16 v[128:143], v[32:33], v[34:35], v[128:143]\n" \
  "s_waitcnt lgkmcnt(6)\n" \
  "v_mfma_f32_16x16x4_4b_bf16 v[144:159], v[32:33], v[36:37], v[144:159]\n" \
  "s_waitcnt lgkmcnt(5)\n" \
  "v_mfma_f32_16x16x4_4b_bf16 v[160:175], v[32:33], v[38:39], v[160:175]\n" \
  "s_waitcnt lgkmcnt(4)\n" \
  "v_mfma_f32_16x16x4_4b_bf16 v[176:191], v[32:33], v[40:41], v[176:191]\n" \
  "s_waitcnt lgkmcnt(3)\n" \
  "v_mfma_f32_16x16x4_4b_bf16 v[192:207], v[32:33], v[42:43], v[192:207]\n" \
  "s_waitcnt lgkmcnt(2)\n" \
  "v_mfma_f32_16x16x4_4b_bf16 v[208:223], v[32:33], v[44:45], v[208:223]\n" \
  "s_waitcnt lgkmcnt(1)\n" \
  "v_mfma_f32_16x16x4_4b_bf16 v[224:239], v[32:33], v[46:47], v[224:239]\n" \
  "s_waitcnt lgkmcnt(0)\n" \
  "v_mfma_f32_16x16x4_4b_bf16 v[240:255], v[32:33], v[48:49], v[240:255]\n" \
  "v_mul_f32 v128, v128, v136\n" \
  "v_mul_f32 v129, v129, v137\n" \
  "v_mul_f32 v130, v130, v138\n" \
  "v_mul_f32 v131, v131, v139\n" \
  "v_mul_f32 v132, v132, v140\n" \
  "v_mul_f32 v133, v133, v141\n" \
  "v_mul_f32 v134, v134, v142\n" \
  "v_mul_f32 v135, v135, v143\n" \
  "v_cvt_pk_bf16_f32 v136, v128, v129\n" \
  "v_cvt_pk_bf16_f32 v137, v130, v131\n" \
  "v_cvt_pk_bf16_f32 v138, v132, v133\n" \
  "v_cvt_pk_bf16_f32 v139, v134, v135\n" \
  "buffer_store_dwordx4 v[136:139], v21, s[36:39], s86 offen offset:0 sc1\n" \
  "v_mul_f32 v144, v144, v152\n" \
  "v_mul_f32 v145, v145, v153\n" \
  "v_mul_f32 v146, v146, v154\n" \
  "v_mul_f32 v147, v147, v155\n" \
  "v_mul_f32 v148, v148, v156\n" \
  "v_mul_f32 v149, v149, v157\n" \
  "v_mul_f32 v150, v150, v158\n" \
  "v_mul_f32 v151, v151, v159\n" \
  "v_cvt_pk_bf16_f32 v152, v144, v145\n" \
  "v_cvt_pk_bf16_f32 v153, v146, v147\n" \
  "v_cvt_pk_bf16_f32 v154, v148, v149\n" \
  "v_cvt_pk_bf16_f32 v155, v150, v151\n" \
  "buffer_store_dwordx4 v[152:155], v21, s[36:39], s86 offen offset:256 sc1\n" \
  "s_add_u32 s86, s86, 147456\n" \
  "v_mul_f32 v160, v160, v168\n" \
  "v_mul_f32 v161, v161, v169\n" \
  "v_mul_f32 v162, v162, v170\n" \
  "v_mul_f32 v163, v163, v171\n" \
  "v_mul_f32 v164, v164, v172\n" \
  "v_mul_f32 v165, v165, v173\n" \
  "v_mul_f32 v166, v166, v174\n" \
  "v_mul_f32 v167, v167, v175\n" \
  "v_cvt_pk_bf16_f32 v168, v160, v161\n" \
  "v_cvt_pk_bf16_f32 v169, v162, v163\n" \
  "v_cvt_pk_bf16_f32 v170, v164, v165\n" \
  "v_cvt_pk_bf16_f32 v171, v166, v167\n" \
  "buffer_store_dwordx4 v[168:171], v21, s[36:39], s86 offen offset:0 sc1\n" \
  "v_mul_f32 v176, v176, v184\n" \
  "v_mul_f32 v177, v177, v185\n" \
  "v_mul_f32 v178, v178, v186\n" \
  "v_mul_f32 v179, v179, v187\n" \
  "v_mul_f32 v180, v180, v188\n" \
  "v_mul_f32 v181, v181, v189\n" \
  "v_mul_f32 v182, v182, v190\n" \
  "v_mul_f32 v183, v183, v191\n" \
  "v_cvt_pk_bf16_f32 v184, v176, v177\n" \
  "v_cvt_pk_bf16_f32 v185, v178, v179\n" \
  "v_cvt_pk_bf16_f32 v186, v180, v181\n" \
  "v_cvt_pk_bf16_f32 v187, v182, v183\n" \
  "buffer_store_dwordx4 v[184:187], v21, s[36:39], s86 offen offset:256 sc1\n" \
  "s_add_u32 s86, s86, 147456\n" \
  "v_mul_f32 v192, v192, v200\n" \
  "v_mul_f32 v193, v193, v201\n" \
  "v_mul_f32 v194, v194, v202\n" \
  "v_mul_f32 v195, v195, v203\n" \
  "v_mul_f32 v196, v196, v204\n" \
  "v_mul_f32 v197, v197, v205\n" \
  "v_mul_f32 v198, v198, v206\n" \
  "v_mul_f32 v199, v199, v207\n" \
  "v_cvt_pk_bf16_f32 v200, v192, v193\n" \
  "v_cvt_pk_bf16_f32 v201, v194, v195\n" \
  "v_cvt_pk_bf16_f32 v202, v196, v197\n" \
  "v_cvt_pk_bf16_f32 v203, v198, v199\n" \
  "s_waitcnt vmcnt(3)\n" \
  "buffer_store_dwordx4 v[200:203], v21, s[36:39], s86 offen offset:0 sc1\n" \
  "v_mul_f32 v208, v208, v216\n" \
  "v_mul_f32 v209, v209, v217\n" \
  "v_mul_f32 v210, v210, v218\n" \
  "v_mul_f32 v211, v211, v219\n" \
  "v_mul_f32 v212, v212, v220\n" \
  "v_mul_f32 v213, v213, v221\n" \
  "v_mul_f32 v214, v214, v222\n" \
  "v_mul_f32 v215, v215, v223\n" \
  "v_cvt_pk_bf16_f32 v216, v208, v209\n" \
  "v_cvt_pk_bf16_f32 v217, v210, v211\n" \
  "v_cvt_pk_bf16_f32 v218, v212, v213\n" \
  "v_cvt_pk_bf16_f32 v219, v214, v215\n" \
  "s_waitcnt vmcnt(3)\n" \
  "buffer_store_dwordx4 v[216:219], v21, s[36:39], s86 offen offset:256 sc1\n" \
  "s_add_u32 s86, s86, 147456\n" \
  "v_mul_f32 v224, v224, v232\n" \
  "v_mul_f32 v225, v225, v233\n" \
  "v_mul_f32 v226, v226, v234\n" \
  "v_mul_f32 v227, v227, v235\n" \
  "v_mul_f32 v228, v228, v236\n" \
  "v_mul_f32 v229, v229, v237\n" \
  "v_mul_f32 v230, v230, v238\n" \
  "v_mul_f32 v231, v231, v239\n" \
  "v_cvt_pk_bf16_f32 v232, v224, v225\n" \
  "v_cvt_pk_bf16_f32 v233, v226, v227\n" \
  "v_cvt_pk_bf16_f32 v234, v228, v229\n" \
  "v_cvt_pk_bf16_f32 v235, v230, v231\n" \
  "s_waitcnt vmcnt(3)\n" \
  "buffer_store_dwordx4 v[232:235], v21, s[36:39], s86 offen offset:0 sc1\n" \
  "v_mul_f32 v240, v240, v248\n" \
  "v_mul_f32 v241, v241, v249\n" \
  "v_mul_f32 v242, v242, v250\n" \
  "v_mul_f32 v243, v243, v251\n" \
  "v_mul_f32 v244, v244, v252\n" \
  "v_mul_f32 v245, v245, v253\n" \
  "v_mul_f32 v246, v246, v254\n" \
  "v_mul_f32 v247, v247, v255\n" \
  "v_cvt_pk_bf16_f32 v248, v240, v241\n" \
  "v_cvt_pk_bf16_f32 v249, v242, v243\n" \
  "v_cvt_pk_bf16_f32 v250, v244, v245\n" \
  "v_cvt_pk_bf16_f32 v251, v246, v247\n" \
  "s_waitcnt vmcnt(3)\n" \
  "buffer_store_dwordx4 v[248:251], v21, s[36:39], s86 offen offset:256 sc1\n" \
  "v_bfe_u32 v20, v0, 3, 3\n" \
  "v_bfe_u32 v21, v0, 4, 1\n" \
  "v_lshl_or_b32 v20, v21, 3, v20\n" \
  "v_and_b32 v21, 7, v0\n" \
  "v_xor_b32 v20, v20, v21\n" \
  "v_bfe_u32 v21, v0, 3, 5\n" \
  "v_lshlrev_b32 v21, 8, v21\n" \
  "v_lshl_or_b32 v20, v20, 4, v21\n" \
  "v_xor_b32 v21, 0x80, v20\n" \
    \
  "s_setprio 0\n" \
  "s_waitcnt vmcnt(0)\n" \
  "s_setprio 2\n" \
  "s_mov_b64 exec, 1\n" \
  "v_mov_b32 v29, 159748\n" \
  "v_mov_b32 v30, 1\n" \
  "ds_add_rtn_u32 v30, v29, v30\n" \
  "v_mov_b32 v29, 0\n" \
  "s_waitcnt lgkmcnt(0)\n" \
  "v_readfirstlane_b32 s63, v30\n" \
  "v_mov_b32 v30, s51\n" \
  "s_cmp_lg_u32 s63, 3\n" \
  "s_cbranch_scc1 .Lfz_sig1\n" \
  "buffer_store_dword v30, v29, s[36:39], s60 offen sc1\n" \
  ".Lfz_sig1:\n" \
  "s_mov_b64 exec, -1\n" \
  "s_setprio 0\n" \
  "buffer_load_dwordx4 v[16:19], v14, s[4:7], s61 offen\n" \
  "buffer_load_dword v28, v27, s[36:39], 0 offen sc1\n" \
  "s_add_u32 m0, s59, 0\n" \
  "s_nop 0\n" \
  "buffer_load_dwordx4 v8, s[32:35], s55 offen lds\n" \
  "buffer_load_dwordx4 v15, s[32:35], s55 offen offset:1024 lds\n" \
  "s_add_u32 s55, s55, 7168\n" \
  "s_add_u32 m0, s59, 8192\n" \
  "s_nop 0\n" \
  "buffer_load_dwordx4 v8, s[32:35], s55 offen lds\n" \
  "buffer_load_dwordx4 v15, s[32:35], s55 offen offset:1024 lds\n" \
  "s_add_u32 s55, s55, 21504\n" \
  "s_waitcnt vmcnt(4)\n" \
  "s_add_u32 m0, s59, 16384\n" \
  "s_nop 0\n" \
  "buffer_load_dwordx4 v8, s[32:35], s55 offen lds\n" \
  "buffer_load_dwordx4 v15, s[32:35], s55 offen offset:1024 lds\n" \
  "s_add_u32 s55, s55, 7168\n" \
  "s_waitcnt vmcnt(4)\n" \
  "s_add_u32 m0, s59, 24576\n" \
  "s_add_u32 s94, s94, 1\n" \
  "buffer_load_dwordx4 v8, s[32:35], s55 offen lds\n" \
  "buffer_load_dwordx4 v15, s[32:35], s55 offen offset:1024 lds\n" \
  "s_cmp_eq_u32 s94, 7\n" \
  "s_cselect_b32 s87, 401408, 0\n" \
  "s_sub_u32 s55, s55, s87\n" \
  "s_add_u32 s55, s55, 21504\n" \
  "s_waitcnt vmcnt(4)\n" \
  "s_add_u32 m0, s59, 32768\n" \
  "s_nop 0\n" \
  "buffer_load_dwordx4 v8, s[32:35], s55 offen lds\n" \
  "buffer_load_dwordx4 v15, s[32:35], s55 offen offset:1024 lds\n" \
  "s_add_u32 s55, s55, 7168\n" \
  "s_waitcnt vmcnt(4)\n" \
  "s_add_u32 m0, s59, 40960\n" \
  "s_nop 0\n" \
  "buffer_load_dwordx4 v8, s[32:35], s55 offen lds\n" \
  "buffer_load_dwordx4 v15, s[32:35], s55 offen offset:1024 lds\n" \
  "s_add_u32 s55, s55, 21504\n" \
  "s_waitcnt vmcnt(4)\n" \
  "s_add_u32 m0, s59, 49152\n" \
  "s_nop 0\n" \
  "buffer_load_dwordx4 v8, s[32:35], s55 offen lds\n" \
  "buffer_load_dwordx4 v15, s[32:35], s55 offen offset:1024 lds\n" \
  "s_add_u32 s55, s55, 7168\n" \
  "s_waitcnt vmcnt(4)\n" \
  "s_add_u32 m0, s59, 57344\n" \
  "s_add_u32 s94, s94, 1\n" \
  "buffer_load_dwordx4 v8, s[32:35], s55 offen lds\n" \
  "buffer_load_dwordx4 v15, s[32:35], s55 offen offset:1024 lds\n" \
  "s_cmp_eq_u32 s94, 7\n" \
  "s_cselect_b32 s87, 401408, 0\n" \
  "s_sub_u32 s55, s55, s87\n" \
  "s_add_u32 s55, s55, 21504\n" \
  "buffer_load_dword v29, v26, s[36:39], 0 offen sc1\n" \
  "s_waitcnt vmcnt(4)\n" \
  "s_add_u32 m0, s59, 65536\n" \
  "s_nop 0\n" \
  "buffer_load_dwordx4 v8, s[32:35], s55 offen lds\n" \
  "buffer_load_dwordx4 v15, s[32:35], s55 offen offset:1024 lds\n" \
  "s_add_u32 s55, s55, 7168\n" \
  "s_waitcnt vmcnt(4)\n" \
  "s_add_u32 m0, s59, 73728\n" \
  "s_nop 0\n" \
  "buffer_load_dwordx4 v8, s[32:35], s55 offen lds\n" \
  "buffer_load_dwordx4 v15, s[32:35], s55 offen offset:1024 lds\n" \
  "s_add_u32 s55, s55, 21504\n" \
  "s_waitcnt vmcnt(4)\n" \
  "s_add_u32 m0, s59, 81920\n" \
  "s_nop 0\n" \
  "buffer_load_dwordx4 v8, s[32:35], s55 offen lds\n" \
  "buffer_load_dwordx4 v15, s[32:35], s55 offen offset:1024 lds\n" \
  "s_add_u32 s55, s55, 7168\n" \
  "s_setprio 3\n" \
  "v_cmp_ne_u32 vcc, s51, v29\n" \
  "s_cbranch_vccnz .Lfz_hps\n" \
  ".Lfz_hpx:\n" \
  "s_setprio 0\n" \
  "s_waitcnt vmcnt(4)\n" \
  "buffer_load_dwordx4 v[48:51], v24, s[36:39], s57 offen offset:0 sc1\n" \
  "buffer_load_dwordx4 v[52:55], v25, s[36:39], s57 offen offset:0 sc1\n" \
  "s_waitcnt vmcnt(4)\n" \
  "buffer_load_dwordx4 v[56:59], v24, s[36:39], s57 offen offset:1024 sc1\n" \
  "buffer_load_dwordx4 v[60:63], v25, s[36:39], s57 offen offset:1024 sc1\n" \
  "s_waitcnt vmcnt(4)\n" \
  "buffer_load_dwordx4 v[64:67], v24, s[36:39], s57 offen offset:2048 sc1\n" \
  "buffer_load_dwordx4 v[68:71], v25, s[36:39], s57 offen offset:2048 sc1\n" \
  "s_waitcnt vmcnt(4)\n" \
  "buffer_load_dwordx4 v[72:75], v24, s[36:39], s57 offen offset:3072 sc1\n" \
  "buffer_load_dwordx4 v[76:79], v25, s[36:39], s57 offen offset:3072 sc1\n" \
  "s_add_u32 s95, s95, 1\n" \
  "s_cmp_eq_u32 s95, 7\n" \
  "s_cselect_b32 s87, 28672, 0\n" \
  "s_add_u32 s57, s57, 4096\n" \
  "s_sub_u32 s57, s57, s87\n" \
  "ds_read_b64_tr_b16 v[32:33], v10 offset:0\n" \
  "ds_read_b64_tr_b16 v[34:35], v10 offset:1024\n" \
  "ds_read_b64_tr_b16 v[36:37], v11 offset:0\n" \
  "ds_read_b64_tr_b16 v[38:39], v11 offset:1024\n" \
  "s_waitcnt lgkmcnt(0)\n" \
  "v_mfma_f32_32x32x16_bf16 v[208:223], v[32:35], v[52:55], 0\n" \
  "ds_read_b64_tr_b16 v[40:41], v10 offset:8192\n" \
  "ds_read_b64_tr_b16 v[42:43], v10 offset:9216\n" \
  "ds_read_b64_tr_b16 v[44:45], v11 offset:8192\n" \
  "ds_read_b64_tr_b16 v[46:47], v11 offset:9216\n" \
  "v_mfma_f32_32x32x16_bf16 v[240:255], v[36:39], v[52:55], 0\n" \
  "s_add_u32 m0, s59, 0\n" \
  "s_add_u32 s94, s94, 1\n" \
  "buffer_load_dwordx4 v8, s[32:35], s55 offen lds\n" \
  "buffer_load_dwordx4 v15, s[32:35], s55 offen offset:1024 lds\n" \
  "s_cmp_eq_u32 s94, 7\n" \
  "s_cselect_b32 s87, 401408, 0\n" \
  "s_sub_u32 s55, s55, s87\n" \
  "s_add_u32 s55, s55, 21504\n" \
  "v_mfma_f32_32x32x16_bf16 v[192:207], v[32:35], v[48:51], 0\n" \
  "s_waitcnt vmcnt(6)\n" \
  "buffer_load_dwordx4 v[80:83], v24, s[36:39], s57 offen offset:0 sc1\n" \
  "buffer_load_dwordx4 v[84:87], v25, s[36:39], s57 offen offset:0 sc1\n" \
  "s_waitcnt vmcnt(6)\n" \
  "buffer_load_dwordx4 v[88:91], v24, s[36:39], s57 offen offset:1024 sc1\n" \
  "buffer_load_dwordx4 v[92:95], v25, s[36:39], s57 offen offset:1024 sc1\n" \
  "v_mfma_f32_32x32x16_bf16 v[224:239], v[36:39], v[48:51], 0\n" \
  "s_waitcnt lgkmcnt(0)\n" \
  "v_mfma_f32_32x32x16_bf16 v[208:223], v[40:43], v[60:63], v[208:223]\n" \
  "ds_read_b64_tr_b16 v[32:33], v10 offset:16384\n" \
  "ds_read_b64_tr_b16 v[34:35], v10 offset:17408\n" \
  "ds_read_b64_tr_b16 v[36:37], v11 offset:16384\n" \
  "ds_read_b64_tr_b16 v[38:39], v11 offset:17408\n" \
  "v_mfma_f32_32x32x16_bf16 v[240:255], v[44:47], v[60:63], v[240:255]\n" \
  "s_waitcnt vmcnt(6)\n" \
  "s_add_u32 m0, s59, 8192\n" \
  "s_nop 0\n" \
  "buffer_load_dwordx4 v8, s[32:35], s55 offen lds\n" \
  "buffer_load_dwordx4 v15, s[32:35], s55 offen offset:1024 lds\n" \
  "s_add_u32 s55, s55, 7168\n" \
  "v_mfma_f32_32x32x16_bf16 v[192:207], v[40:43], v[56:59], v[192:207]\n" \
  "s_waitcnt vmcnt(6)\n" \
  "buffer_load_dwordx4 v[96:99], v24, s[36:39], s57 offen offset:2048 sc1\n" \
  "buffer_load_dwordx4 v[100:103], v25, s[36:39], s57 offen offset:2048 sc1\n" \
  "s_waitcnt vmcnt(6)\n" \
  "buffer_load_dwordx4 v[104:107], v24, s[36:39], s57 offen offset:3072 sc1\n" \
  "buffer_load_dwordx4 v[108:111], v25, s[36:39], s57 offen offset:3072 sc1\n" \
  "s_add_u32 s95, s95, 1\n" \
  "s_cmp_eq_u32 s95, 7\n" \
  "s_cselect_b32 s87, 28672, 0\n" \
  "s_add_u32 s57, s57, 4096\n" \
  "s_sub_u32 s57, s57, s87\n" \
  "v_mfma_f32_32x32x16_bf16 v[224:239], v[44:47], v[56:59], v[224:239]\n" \
  "s_waitcnt lgkmcnt(0)\n" \
  "v_mfma_f32_32x32x16_bf16 v[208:223], v[32:35], v[68:71], v[208:223]\n" \
  "ds_read_b64_tr_b16 v[40:41], v10 offset:24576\n" \
  "ds_read_b64_tr_b16 v[42:43], v10 offset:25600\n" \
  "ds_read_b64_tr_b16 v[44:45], v11 offset:24576\n" \
  "ds_read_b64_tr_b16 v[46:47], v11 offset:25600\n" \
  "v_mfma_f32_32x32x16_bf16 v[240:255], v[36:39], v[68:71], v[240:255]\n" \
  "s_waitcnt vmcnt(6)\n" \
  "s_add_u32 m0, s59, 16384\n" \
  "s_nop 0\n" \
  "buffer_load_dwordx4 v8, s[32:35], s55 offen lds\n" \
  "buffer_load_dwordx4 v15, s[32:35], s55 offen offset:1024 lds\n" \
  "s_add_u32 s55, s55, 21504\n" \
  "v_mfma_f32_32x32x16_bf16 v[192:207], v[32:35], v[64:67], v[192:207]\n" \
  "s_waitcnt vmcnt(6)\n" \
  "buffer_load_dwordx4 v[112:115], v24, s[36:39], s57 offen offset:0 sc1\n" \
  "buffer_load_dwordx4 v[116:119], v25, s[36:39], s57 offen offset:0 sc1\n" \
  "s_waitcnt vmcnt(6)\n" \
  "buffer_load_dwordx4 v[120:123], v24, s[36:39], s57 offen offset:1024 sc1\n" \
  "buffer_load_dwordx4 v[124:127], v25, s[36:39], s57 offen offset:1024 sc1\n" \
  "v_mfma_f32_32x32x16_bf16 v[224:239], v[36:39], v[64:67], v[224:239]\n" \
  "s_waitcnt lgkmcnt(0)\n" \
  "v_mfma_f32_32x32x16_bf16 v[208:223], v[40:43], v[76:79], v[208:223]\n" \
  "ds_read_b64_tr_b16 v[32:33], v10 offset:32768\n" \
  "ds_read_b64_tr_b16 v[34:35], v10 offset:33792\n" \
  "ds_read_b64_tr_b16 v[36:37], v11 offset:32768\n" \
  "ds_read_b64_tr_b16 v[38:39], v11 offset:33792\n" \
  "v_mfma_f32_32x32x16_bf16 v[240:255], v[44:47], v[76:79], v[240:255]\n" \
  "s_waitcnt vmcnt(6)\n" \
  "s_add_u32 m0, s59, 24576\n" \
  "s_nop 0\n" \
  "buffer_load_dwordx4 v8, s[32:35], s55 offen lds\n" \
  "buffer_load_dwordx4 v15, s[32:35], s55 offen offset:1024 lds\n" \
  "s_add_u32 s55, s55, 7168\n" \
  "v_mfma_f32_32x32x16_bf16 v[192:207], v[40:43], v[72:75], v[192:207]\n" \
  "s_waitcnt vmcnt(6)\n" \
  "buffer_load_dwordx4 v[128:131], v24, s[36:39], s57 offen offset:2048 sc1\n" \
  "buffer_load_dwordx4 v[132:135], v25, s[36:39], s57 offen offset:2048 sc1\n" \
  "s_waitcnt vmcnt(6)\n" \
  "buffer_load_dwordx4 v[136:139], v24, s[36:39], s57 offen offset:3072 sc1\n" \
  "buffer_load_dwordx4 v[140:143], v25, s[36:39], s57 offen offset:3072 sc1\n" \
  "s_add_u32 s95, s95, 1\n" \
  "s_cmp_eq_u32 s95, 7\n" \
  "s_cselect_b32 s87, 28672, 0\n" \
  "s_add_u32 s57, s57, 4096\n" \
  "s_sub_u32 s57, s57, s87\n" \
  "v_mfma_f32_32x32x16_bf16 v[224:239], v[44:47], v[72:75], v[224:239]\n" \
  "s_waitcnt lgkmcnt(0)\n" \
  "v_mfma_f32_32x32x16_bf16 v[208:223], v[32:35], v[84:87], v[208:223]\n" \
  "ds_read_b64_tr_b16 v[40:41], v10 offset:40960\n" \
  "ds_read_b64_tr_b16 v[42:43], v10 offset:41984\n" \
  "ds_read_b64_tr_b16 v[44:45], v11 offset:40960\n" \
  "ds_read_b64_tr_b16 v[46:47], v11 offset:41984\n" \
  "v_mfma_f32_32x32x16_bf16 v[240:255], v[36:39], v[84:87], v[240:255]\n" \
  "s_waitcnt vmcnt(6)\n" \
  "s_add_u32 m0, s59, 32768\n" \
  "s_add_u32 s94, s94, 1\n" \
  "buffer_load_dwordx4 v8, s[32:35], s55 offen lds\n" \
  "buffer_load_dwordx4 v15, s[32:35], s55 offen offset:1024 lds\n" \
  "s_cmp_eq_u32 s94, 7\n" \
  "s_cselect_b32 s87, 401408, 0\n" \
  "s_sub_u32 s55, s55, s87\n" \
  "s_add_u32 s55, s55, 21504\n" \
  "v_mfma_f32_32x32x16_bf16 v[192:207], v[32:35], v[80:83], v[192:207]\n" \
  "s_waitcnt vmcnt(6)\n" \
  "buffer_load_dwordx4 v[144:147], v24, s[36:39], s57 offen offset:0 sc1\n" \
  "buffer_load_dwordx4 v[148:151], v25, s[36:39], s57 offen offset:0 sc1\n" \
  "s_waitcnt vmcnt(6)\n" \
  "buffer_load_dwordx4 v[152:155], v24, s[36:39], s57 offen offset:1024 sc1\n" \
  "buffer_load_dwordx4 v[156:159], v25, s[36:39], s57 offen offset:1024 sc1\n" \
  "v_mfma_f32_32x32x16_bf16 v[224:239], v[36:39], v[80:83], v[224:239]\n" \
  "s_waitcnt lgkmcnt(0)\n" \
  "v_mfma_f32_32x32x16_bf16 v[208:223], v[40:43], v[92:95], v[208:223]\n" \
  "ds_read_b64_tr_b16 v[32:33], v10 offset:49152\n" \
  "ds_read_b64_tr_b16 v[34:35], v10 offset:50176\n" \
  "ds_read_b64_tr_b16 v[36:37], v11 offset:49152\n" \
  "ds_read_b64_tr_b16 v[38:39], v11 offset:50176\n" \
  "v_mfma_f32_32x32x16_bf16 v[240:255], v[44:47], v[92:95], v[240:255]\n" \
  "s_waitcnt vmcnt(6)\n" \
  "s_add_u32 m0, s59, 40960\n" \
  "s_nop 0\n" \
  "buffer_load_dwordx4 v8, s[32:35], s55 offen lds\n" \
  "buffer_load_dwordx4 v15, s[32:35], s55 offen offset:1024 lds\n" \
  "s_add_u32 s55, s55, 7168\n" \
  "v_mfma_f32_32x32x16_bf16 v[192:207], v[40:43], v[88:91], v[192:207]\n" \
  "s_waitcnt vmcnt(6)\n" \
  "buffer_load_dwordx4 v[160:163], v24, s[36:39], s57 offen offset:2048 sc1\n" \
  "buffer_load_dwordx4 v[164:167], v25, s[36:39], s57 offen offset:2048 sc1\n" \
  "s_waitcnt vmcnt(6)\n" \
  "buffer_load_dwordx4 v[48:51], v24, s[36:39], s57 offen offset:3072 sc1\n" \
  "buffer_load_dwordx4 v[52:55], v25, s[36:39], s57 offen offset:3072 sc1\n" \
  "s_add_u32 s95, s95, 1\n" \
  "s_cmp_eq_u32 s95, 7\n" \
  "s_cselect_b32 s87, 28672, 0\n" \
  "s_add_u32 s57, s57, 4096\n" \
  "s_sub_u32 s57, s57, s87\n" \
  "v_mfma_f32_32x32x16_bf16 v[224:239], v[44:47], v[88:91], v[224:239]\n" \
  "s_waitcnt lgkmcnt(0)\n" \
  "v_mfma_f32_32x32x16_bf16 v[208:223], v[32:35], v[100:103], v[208:223]\n" \
  "ds_read_b64_tr_b16 v[40:41], v12 offset:0\n" \
  "ds_read_b64_tr_b16 v[42:43], v12 offset:1024\n" \
  "ds_read_b64_tr_b16 v[44:45], v13 offset:0\n" \
  "ds_read_b64_tr_b16 v[46:47], v13 offset:1024\n" \
  "v_mfma_f32_32x32x16_bf16 v[240:255], v[36:39], v[100:103], v[240:255]\n" \
  "s_waitcnt vmcnt(6)\n" \
  "s_add_u32 m0, s59, 49152\n" \
  "s_nop 0\n" \
  "buffer_load_dwordx4 v8, s[32:35], s55 offen lds\n" \
  "buffer_load_dwordx4 v15, s[32:35], s55 offen offset:1024 lds\n" \
  "s_add_u32 s55, s55, 21504\n" \
  "v_mfma_f32_32x32x16_bf16 v[192:207], v[32:35], v[96:99], v[192:207]\n" \
  "s_waitcnt vmcnt(6)\n" \
  "buffer_load_dwordx4 v[56:59], v24, s[36:39], s57 offen offset:0 sc1\n" \
  "buffer_load_dwordx4 v[60:63], v25, s[36:39], s57 offen offset:0 sc1\n" \
  "s_waitcnt vmcnt(6)\n" \
  "buffer_load_dwordx4 v[64:67], v24, s[36:39], s57 offen offset:1024 sc1\n" \
  "buffer_load_dwordx4 v[68:71], v25, s[36:39], s57 offen offset:1024 sc1\n" \
  "v_mfma_f32_32x32x16_bf16 v[224:239], v[36:39], v[96:99], v[224:239]\n" \
  "s_waitcnt lgkmcnt(0)\n" \
  "v_mfma_f32_32x32x16_bf16 v[208:223], v[40:43], v[108:111], v[208:223]\n" \
  "ds_read_b64_tr_b16 v[32:33], v12 offset:8192\n" \
  "ds_read_b64_tr_b16 v[34:35], v12 offset:9216\n" \
  "ds_read_b64_tr_b16 v[36:37], v13 offset:8192\n" \
  "ds_read_b64_tr_b16 v[38:39], v13 offset:9216\n" \
  "v_mfma_f32_32x32x16_bf16 v[240:255], v[44:47], v[108:111], v[240:255]\n" \
  "s_waitcnt vmcnt(6)\n" \
  "s_add_u32 m0, s59, 57344\n" \
  "s_nop 0\n" \
  "buffer_load_dwordx4 v8, s[32:35], s55 offen lds\n" \
  "buffer_load_dwordx4 v15, s[32:35], s55 offen offset:1024 lds\n" \
  "s_add_u32 s55, s55, 7168\n" \
  "v_mfma_f32_32x32x16_bf16 v[192:207], v[40:43], v[104:107], v[192:207]\n" \
  "s_waitcnt vmcnt(6)\n" \
  "buffer_load_dwordx4 v[72:75], v24, s[36:39], s57 offen offset:2048 sc1\n" \
  "buffer_load_dwordx4 v[76:79], v25, s[36:39], s57 offen offset:2048 sc1\n" \
  "s_waitcnt vmcnt(6)\n" \
  "buffer_load_dwordx4 v[80:83], v24, s[36:39], s57 offen offset:3072 sc1\n" \
  "buffer_load_dwordx4 v[84:87], v25, s[36:39], s57 offen offset:3072 sc1\n" \
  "s_add_u32 s95, s95, 1\n" \
  "s_cmp_eq_u32 s95, 7\n" \
  "s_cselect_b32 s87, 28672, 0\n" \
  "s_add_u32 s57, s57, 4096\n" \
  "s_sub_u32 s57, s57, s87\n" \
  "v_mfma_f32_32x32x16_bf16 v[224:239], v[44:47], v[104:107], v[224:239]\n" \
  "s_waitcnt lgkmcnt(0)\n" \
  "v_mfma_f32_32x32x16_bf16 v[208:223], v[32:35], v[116:119], v[208:223]\n" \
  "ds_read_b64_tr_b16 v[40:41], v12 offset:16384\n" \
  "ds_read_b64_tr_b16 v[42:43], v12 offset:17408\n" \
  "ds_read_b64_tr_b16 v[44:45], v13 offset:16384\n" \
  "ds_read_b64_tr_b16 v[46:47], v13 offset:17408\n" \
  "v_mfma_f32_32x32x16_bf16 v[240:255], v[36:39], v[116:119], v[240:255]\n" \
  "s_waitcnt vmcnt(6)\n" \
  "s_add_u32 m0, s59, 65536\n" \
  "s_add_u32 s94, s94, 1\n" \
  "buffer_load_dwordx4 v8, s[32:35], s55 offen lds\n" \
  "buffer_load_dwordx4 v15, s[32:35], s55 offen offset:1024 lds\n" \
  "s_cmp_eq_u32 s94, 7\n" \
  "s_cselect_b32 s87, 401408, 0\n" \
  "s_sub_u32 s55, s55, s87\n" \
  "s_add_u32 s55, s55, 21504\n" \
  "v_mfma_f32_32x32x16_bf16 v[192:207], v[32:35], v[112:115], v[192:207]\n" \
  "s_waitcnt vmcnt(6)\n" \
  "buffer_load_dwordx4 v[88:91], v24, s[36:39], s57 offen offset:0 sc1\n" \
  "buffer_load_dwordx4 v[92:95], v25, s[36:39], s57 offen offset:0 sc1\n" \
  "s_waitcnt vmcnt(6)\n" \
  "buffer_load_dwordx4 v[96:99], v24, s[36:39], s57 offen offset:1024 sc1\n" \
  "buffer_load_dwordx4 v[100:103], v25, s[36:39], s57 offen offset:1024 sc1\n" \
  "v_mfma_f32_32x32x16_bf16 v[224:239], v[36:39], v[112:115], v[224:239]\n" \
  "s_waitcnt lgkmcnt(0)\n" \
  "v_mfma_f32_32x32x16_bf16 v[208:223], v[40:43], v[124:127], v[208:223]\n" \
  "ds_read_b64_tr_b16 v[32:33], v12 offset:24576\n" \
  "ds_read_b64_tr_b16 v[34:35], v12 offset:25600\n" \
  "ds_read_b64_tr_b16 v[36:37], v13 offset:24576\n" \
  "ds_read_b64_tr_b16 v[38:39], v13 offset:25600\n" \
  "v_mfma_f32_32x32x16_bf16 v[240:255], v[44:47], v[124:127], v[240:255]\n" \
  "s_waitcnt vmcnt(6)\n" \
  "s_add_u32 m0, s59, 73728\n" \
  "s_nop 0\n" \
  "buffer_load_dwordx4 v8, s[32:35], s55 offen lds\n" \
  "buffer_load_dwordx4 v15, s[32:35], s55 offen offset:1024 lds\n" \
  "s_add_u32 s55, s55, 7168\n" \
  "v_mfma_f32_32x32x16_bf16 v[192:207], v[40:43], v[120:123], v[192:207]\n" \
  "s_waitcnt vmcnt(6)\n" \
  "buffer_load_dwordx4 v[104:107], v24, s[36:39], s57 offen offset:2048 sc1\n" \
  "buffer_load_dwordx4 v[108:111], v25, s[36:39], s57 offen offset:2048 sc1\n" \
  "s_waitcnt vmcnt(6)\n" \
  "buffer_load_dwordx4 v[112:115], v24, s[36:39], s57 offen offset:3072 sc1\n" \
  "buffer_load_dwordx4 v[116:119], v25, s[36:39], s57 offen offset:3072 sc1\n" \
  "s_add_u32 s95, s95, 1\n" \
  "s_cmp_eq_u32 s95, 7\n" \
  "s_cselect_b32 s87, 28672, 0\n" \
  "s_add_u32 s57, s57, 4096\n" \
  "s_sub_u32 s57, s57, s87\n" \
  "v_mfma_f32_32x32x16_bf16 v[224:239], v[44:47], v[120:123], v[224:239]\n" \
  "s_waitcnt lgkmcnt(0)\n" \
  "v_mfma_f32_32x32x16_bf16 v[208:223], v[32:35], v[132:135], v[208:223]\n" \
  "ds_read_b64_tr_b16 v[40:41], v10 offset:0\n" \
  "ds_read_b64_tr_b16 v[42:43], v10 offset:1024\n" \
  "ds_read_b64_tr_b16 v[44:45], v11 offset:0\n" \
  "ds_read_b64_tr_b16 v[46:47], v11 offset:1024\n" \
  "v_mfma_f32_32x32x16_bf16 v[240:255], v[36:39], v[132:135], v[240:255]\n" \
  "s_waitcnt vmcnt(6)\n" \
  "s_add_u32 m0, s59, 81920\n" \
  "s_nop 0\n" \
  "buffer_load_dwordx4 v8, s[32:35], s55 offen lds\n" \
  "buffer_load_dwordx4 v15, s[32:35], s55 offen offset:1024 lds\n" \
  "s_add_u32 s55, s55, 21504\n" \
  "v_mfma_f32_32x32x16_bf16 v[192:207], v[32:35], v[128:131], v[192:207]\n" \
  "s_waitcnt vmcnt(6)\n" \
  "buffer_load_dwordx4 v[120:123], v24, s[36:39], s57 offen offset:0 sc1\n" \
  "buffer_load_dwordx4 v[124:127], v25, s[36:39], s57 offen offset:0 sc1\n" \
  "v_mfma_f32_32x32x16_bf16 v[224:239], v[36:39], v[128:131], v[224:239]\n" \
  "s_waitcnt lgkmcnt(0)\n" \
  "v_mfma_f32_32x32x16_bf16 v[208:223], v[40:43], v[140:143], v[208:223]\n" \
  "ds_read_b64_tr_b16 v[32:33], v10 offset:8192\n" \
  "ds_read_b64_tr_b16 v[34:35], v10 offset:9216\n" \
  "ds_read_b64_tr_b16 v[36:37], v11 offset:8192\n" \
  "ds_read_b64_tr_b16 v[38:39], v11 offset:9216\n" \
  "v_mfma_f32_32x32x16_bf16 v[240:255], v[44:47], v[140:143], v[240:255]\n" \
  "s_waitcnt vmcnt(6)\n" \
  "s_add_u32 m0, s59, 0\n" \
  "s_nop 0\n" \
  "buffer_load_dwordx4 v8, s[32:35], s55 offen lds\n" \
  "buffer_load_dwordx4 v15, s[32:35], s55 offen offset:1024 lds\n" \
  "s_add_u32 s55, s55, 7168\n" \
  "v_mfma_f32_32x32x16_bf16 v[192:207], v[40:43], v[136:139], v[192:207]\n" \
  "s_waitcnt vmcnt(6)\n" \
  "buffer_load_dwordx4 v[128:131], v24, s[36:39], s57 offen offset:1024 sc1\n" \
  "buffer_load_dwordx4 v[132:135], v25, s[36:39], s57 offen offset:1024 sc1\n" \
  "v_mfma_f32_32x32x16_bf16 v[224:239], v[44:47], v[136:139], v[224:239]\n" \
  "s_waitcnt lgkmcnt(0)\n" \
  "v_mfma_f32_32x32x16_bf16 v[208:223], v[32:35], v[148:151], v[208:223]\n" \
  "ds_read_b64_tr_b16 v[40:41], v10 offset:16384\n" \
  "ds_read_b64_tr_b16 v[42:43], v10 offset:17408\n" \
  "ds_read_b64_tr_b16 v[44:45], v11 offset:16384\n" \
  "ds_read_b64_tr_b16 v[46:47], v11 offset:17408\n" \
  "v_mfma_f32_32x32x16_bf16 v[240:255], v[36:39], v[148:151], v[240:255]\n" \
  "s_add_u32 m0, s59, 8192\n" \
  "s_add_u32 s94, s94, 1\n" \
  "buffer_load_dwordx4 v8, s[32:35], s55 offen lds\n" \
  "buffer_load_dwordx4 v15, s[32:35], s55 offen offset:1024 lds\n" \
  "s_cmp_eq_u32 s94, 7\n" \
  "s_cselect_b32 s87, 401408, 0\n" \
  "s_sub_u32 s55, s55, s87\n" \
  "s_add_u32 s55, s55, 21504\n" \
  "v_mfma_f32_32x32x16_bf16 v[192:207], v[32:35], v[144:147], v[192:207]\n" \
  "buffer_load_dwordx4 v[136:139], v24, s[36:39], s57 offen offset:2048 sc1\n" \
  "buffer_load_dwordx4 v[140:143], v25, s[36:39], s57 offen offset:2048 sc1\n" \
  "v_mfma_f32_32x32x16_bf16 v[224:239], v[36:39], v[144:147], v[224:239]\n" \
  "s_waitcnt lgkmcnt(0)\n" \
  "v_mfma_f32_32x32x16_bf16 v[208:223], v[40:43], v[156:159], v[208:223]\n" \
  "ds_read_b64_tr_b16 v[32:33], v10 offset:24576\n" \
  "ds_read_b64_tr_b16 v[34:35], v10 offset:25600\n" \
  "ds_read_b64_tr_b16 v[36:37], v11 offset:24576\n" \
  "ds_read_b64_tr_b16 v[38:39], v11 offset:25600\n" \
  "v_mfma_f32_32x32x16_bf16 v[240:255], v[44:47], v[156:159], v[240:255]\n" \
  "s_add_u32 m0, s59, 16384\n" \
  "s_nop 0\n" \
  "buffer_load_dwordx4 v8, s[32:35], s55 offen lds\n" \
  "buffer_load_dwordx4 v15, s[32:35], s55 offen offset:1024 lds\n" \
  "s_add_u32 s55, s55, 7168\n" \
  "v_mfma_f32_32x32x16_bf16 v[192:207], v[40:43], v[152:155], v[192:207]\n" \
  "buffer_load_dwordx4 v[144:147], v24, s[36:39], s57 offen offset:3072 sc1\n" \
  "buffer_load_dwordx4 v[148:151], v25, s[36:39], s57 offen offset:3072 sc1\n" \
  "s_add_u32 s95, s95, 1\n" \
  "s_cmp_eq_u32 s95, 7\n" \
  "s_cselect_b32 s87, 28672, 0\n" \
  "s_add_u32 s57, s57, 4096\n" \
  "s_sub_u32 s57, s57, s87\n" \
  "v_mfma_f32_32x32x16_bf16 v[224:239], v[44:47], v[152:155], v[224:239]\n" \
  "s_waitcnt lgkmcnt(0)\n" \
  "v_mfma_f32_32x32x16_bf16 v[208:223], v[32:35], v[164:167], v[208:223]\n" \
  "ds_read_b64_tr_b16 v[40:41], v10 offset:32768\n" \
  "ds_read_b64_tr_b16 v[42:43], v10 offset:33792\n" \
  "ds_read_b64_tr_b16 v[44:45], v11 offset:32768\n" \
  "ds_read_b64_tr_b16 v[46:47], v11 offset:33792\n" \
  "v_mfma_f32_32x32x16_bf16 v[240:255], v[36:39], v[164:167], v[240:255]\n" \
  "s_add_u32 m0, s59, 24576\n" \
  "s_nop 0\n" \
  "buffer_load_dwordx4 v8, s[32:35], s55 offen lds\n" \
  "buffer_load_dwordx4 v15, s[32:35], s55 offen offset:1024 lds\n" \
  "s_add_u32 s55, s55, 21504\n" \
  "v_mfma_f32_32x32x16_bf16 v[192:207], v[32:35], v[160:163], v[192:207]\n" \
  "v_mfma_f32_32x32x16_bf16 v[224:239], v[36:39], v[160:163], v[224:239]\n" \
  "s_waitcnt lgkmcnt(0)\n" \
  "v_mfma_f32_32x32x16_bf16 v[208:223], v[40:43], v[52:55], v[208:223]\n" \
  "ds_read_b64_tr_b16 v[32:33], v10 offset:40960\n" \
  "ds_read_b64_tr_b16 v[34:35], v10 offset:41984\n" \
  "ds_read_b64_tr_b16 v[36:37], v11 offset:40960\n" \
  "ds_read_b64_tr_b16 v[38:39], v11 offset:41984\n" \
  "v_mfma_f32_32x32x16_bf16 v[240:255], v[44:47], v[52:55], v[240:255]\n" \
  "s_add_u32 m0, s59, 32768\n" \
  "s_nop 0\n" \
  "buffer_load_dwordx4 v8, s[32:35], s55 offen lds\n" \
  "buffer_load_dwordx4 v15, s[32:35], s55 offen offset:1024 lds\n" \
  "s_add_u32 s55, s55, 7168\n" \
  "v_mfma_f32_32x32x16_bf16 v[192:207], v[40:43], v[48:51], v[192:207]\n" \
  "v_mfma_f32_32x32x16_bf16 v[224:239], v[44:47], v[48:51], v[224:239]\n" \
  "s_waitcnt lgkmcnt(0)\n" \
  "v_mfma_f32_32x32x16_bf16 v[208:223], v[32:35], v[60:63], v[208:223]\n" \
  "ds_read_b64_tr_b16 v[40:41], v10 offset:49152\n" \
  "ds_read_b64_tr_b16 v[42:43], v10 offset:50176\n" \
  "ds_read_b64_tr_b16 v[44:45], v11 offset:49152\n" \
  "ds_read_b64_tr_b16 v[46:47], v11 offset:50176\n" \
  "v_mfma_f32_32x32x16_bf16 v[240:255], v[36:39], v[60:63], v[240:255]\n" \
  "s_add_u32 m0, s59, 40960\n" \
  "s_add_u32 s94, s94, 1\n" \
  "buffer_load_dwordx4 v8, s[32:35], s55 offen lds\n" \
  "buffer_load_dwordx4 v15, s[32:35], s55 offen offset:1024 lds\n" \
  "s_cmp_eq_u32 s94, 7\n" \
  "s_cselect_b32 s87, 401408, 0\n" \
  "s_sub_u32 s55, s55, s87\n" \
  "s_add_u32 s55, s55, 21504\n" \
  "v_mfma_f32_32x32x16_bf16 v[192:207], v[32:35], v[56:59], v[192:207]\n" \
  "v_mfma_f32_32x32x16_bf16 v[224:239], v[36:39], v[56:59], v[224:239]\n" \
  "s_waitcnt lgkmcnt(0)\n" \
  "v_mfma_f32_32x32x16_bf16 v[208:223], v[40:43], v[68:71], v[208:223]\n" \
  "ds_read_b64_tr_b16 v[32:33], v12 offset:0\n" \
  "ds_read_b64_tr_b16 v[34:35], v12 offset:1024\n" \
  "ds_read_b64_tr_b16 v[36:37], v13 offset:0\n" \
  "ds_read_b64_tr_b16 v[38:39], v13 offset:1024\n" \
  "v_mfma_f32_32x32x16_bf16 v[240:255], v[44:47], v[68:71], v[240:255]\n" \
  "s_add_u32 m0, s59, 49152\n" \
  "s_nop 0\n" \
  "buffer_load_dwordx4 v8, s[20:23], s56 offen lds\n" \
  "buffer_load_dwordx4 v9, s[20:23], s56 offen offset:1024 lds\n" \
  "s_add_u32 s56, s56, 14336\n" \
  "v_mfma_f32_32x32x16_bf16 v[192:207], v[40:43], v[64:67], v[192:207]\n" \
  "v_mfma_f32_32x32x16_bf16 v[224:239], v[44:47], v[64:67], v[224:239]\n" \
  "s_waitcnt lgkmcnt(0)\n" \
  "v_mfma_f32_32x32x16_bf16 v[208:223], v[32:35], v[76:79], v[208:223]\n" \
  "ds_read_b64_tr_b16 v[40:41], v12 offset:8192\n" \
  "ds_read_b64_tr_b16 v[42:43], v12 offset:9216\n" \
  "ds_read_b64_tr_b16 v[44:45], v13 offset:8192\n" \
  "ds_read_b64_tr_b16 v[46:47], v13 offset:9216\n" \
  "v_mfma_f32_32x32x16_bf16 v[240:255], v[36:39], v[76:79], v[240:255]\n" \
  "s_add_u32 m0, s59, 57344\n" \
  "s_nop 0\n" \
  "buffer_load_dwordx4 v8, s[20:23], s56 offen lds\n" \
  "buffer_load_dwordx4 v7, s[20:23], s56 offen offset:1024 lds\n" \
  "s_add_u32 s56, s56, 10752\n" \
  "v_mfma_f32_32x32x16_bf16 v[192:207], v[32:35], v[72:75], v[192:207]\n" \
  "v_mfma_f32_32x32x16_bf16 v[224:239], v[36:39], v[72:75], v[224:239]\n" \
  "s_waitcnt lgkmcnt(0)\n" \
  "v_mfma_f32_32x32x16_bf16 v[208:223], v[40:43], v[84:87], v[208:223]\n" \
  "ds_read_b64_tr_b16 v[32:33], v12 offset:16384\n" \
  "ds_read_b64_tr_b16 v[34:35], v12 offset:17408\n" \
  "ds_read_b64_tr_b16 v[36:37], v13 offset:16384\n" \
  "ds_read_b64_tr_b16 v[38:39], v13 offset:17408\n" \
  "v_mfma_f32_32x32x16_bf16 v[240:255], v[44:47], v[84:87], v[240:255]\n" \
  "s_add_u32 m0, s59, 65536\n" \
  "s_nop 0\n" \
  "buffer_load_dwordx4 v8, s[20:23], s56 offen lds\n" \
  "buffer_load_dwordx4 v9, s[20:23], s56 offen offset:1024 lds\n" \
  "s_add_u32 s56, s56, 14336\n" \
  "v_mfma_f32_32x32x16_bf16 v[192:207], v[40:43], v[80:83], v[192:207]\n" \
  "v_mfma_f32_32x32x16_bf16 v[224:239], v[44:47], v[80:83], v[224:239]\n" \
  "s_waitcnt vmcnt(26)\n" \
  "s_waitcnt lgkmcnt(0)\n" \
  "v_mfma_f32_32x32x16_bf16 v[208:223], v[32:35], v[92:95], v[208:223]\n" \
  "ds_read_b64_tr_b16 v[40:41], v12 offset:24576\n" \
  "ds_read_b64_tr_b16 v[42:43], v12 offset:25600\n" \
  "ds_read_b64_tr_b16 v[44:45], v13 offset:24576\n" \
  "ds_read_b64_tr_b16 v[46:47], v13 offset:25600\n" \
  "v_mfma_f32_32x32x16_bf16 v[240:255], v[36:39], v[92:95], v[240:255]\n" \
  "s_add_u32 m0, s59, 73728\n" \
  "s_nop 0\n" \
  "buffer_load_dwordx4 v8, s[20:23], s56 offen lds\n" \
  "buffer_load_dwordx4 v7, s[20:23], s56 offen offset:1024 lds\n" \
  "s_add_u32 s56, s56, 10752\n" \
  "v_mfma_f32_32x32x16_bf16 v[192:207], v[32:35], v[88:91], v[192:207]\n" \
  "v_mfma_f32_32x32x16_bf16 v[224:239], v[36:39], v[88:91], v[224:239]\n" \
  "s_waitcnt vmcnt(24)\n" \
  "s_waitcnt lgkmcnt(0)\n" \
  "v_mfma_f32_32x32x16_bf16 v[208:223], v[40:43], v[100:103], v[208:223]\n" \
  "ds_read_b64_tr_b16 v[32:33], v10 offset:0\n" \
  "ds_read_b64_tr_b16 v[34:35], v10 offset:1024\n" \
  "ds_read_b64_tr_b16 v[36:37], v11 offset:0\n" \
  "ds_read_b64_tr_b16 v[38:39], v11 offset:1024\n" \
  "v_mfma_f32_32x32x16_bf16 v[240:255], v[44:47], v[100:103], v[240:255]\n" \
  "s_add_u32 m0, s59, 81920\n" \
  "s_nop 0\n" \
  "buffer_load_dwordx4 v8, s[20:23], s56 offen lds\n" \
  "buffer_load_dwordx4 v9, s[20:23], s56 offen offset:1024 lds\n" \
  "s_add_u32 s56, s56, 14336\n" \
  "v_mfma_f32_32x32x16_bf16 v[192:207], v[40:43], v[96:99], v[192:207]\n" \
  "v_mfma_f32_32x32x16_bf16 v[224:239], v[44:47], v[96:99], v[224:239]\n" \
  "s_waitcnt vmcnt(22)\n" \
  "s_waitcnt lgkmcnt(0)\n" \
  "v_mfma_f32_32x32x16_bf16 v[208:223], v[32:35], v[108:111], v[208:223]\n" \
  "ds_read_b64_tr_b16 v[40:41], v10 offset:8192\n" \
  "ds_read_b64_tr_b16 v[42:43], v10 offset:9216\n" \
  "ds_read_b64_tr_b16 v[44:45], v11 offset:8192\n" \
  "ds_read_b64_tr_b16 v[46:47], v11 offset:9216\n" \
  "v_mfma_f32_32x32x16_bf16 v[240:255], v[36:39], v[108:111], v[240:255]\n" \
  "s_add_u32 m0, s59, 0\n" \
  "s_nop 0\n" \
  "buffer_load_dwordx4 v8, s[20:23], s56 offen lds\n" \
  "buffer_load_dwordx4 v7, s[20:23], s56 offen offset:1024 lds\n" \
  "s_add_u32 s56, s56, 10752\n" \
  "v_mfma_f32_32x32x16_bf16 v[192:207], v[32:35], v[104:107], v[192:207]\n" \
  "v_mfma_f32_32x32x16_bf16 v[224:239], v[36:39], v[104:107], v[224:239]\n" \
  "s_waitcnt vmcnt(20)\n" \
  "s_waitcnt lgkmcnt(0)\n" \
  "v_mfma_f32_32x32x16_bf16 v[208:223], v[40:43], v[116:119], v[208:223]\n" \
  "ds_read_b64_tr_b16 v[32:33], v10 offset:16384\n" \
  "ds_read_b64_tr_b16 v[34:35], v10 offset:17408\n" \
  "ds_read_b64_tr_b16 v[36:37], v11 offset:16384\n" \
  "ds_read_b64_tr_b16 v[38:39], v11 offset:17408\n" \
  "v_mfma_f32_32x32x16_bf16 v[240:255], v[44:47], v[116:119], v[240:255]\n" \
  "s_add_u32 m0, s59, 8192\n" \
  "s_nop 0\n" \
  "buffer_load_dwordx4 v8, s[20:23], s56 offen lds\n" \
  "buffer_load_dwordx4 v9, s[20:23], s56 offen offset:1024 lds\n" \
  "s_add_u32 s56, s56, 14336\n" \
  "v_mfma_f32_32x32x16_bf16 v[192:207], v[40:43], v[112:115], v[192:207]\n" \
  "v_mfma_f32_32x32x16_bf16 v[224:239], v[44:47], v[112:115], v[224:239]\n" \
  "s_waitcnt vmcnt(18)\n" \
  "s_waitcnt lgkmcnt(0)\n" \
  "v_mfma_f32_32x32x16_bf16 v[208:223], v[32:35], v[124:127], v[208:223]\n" \
  "ds_read_b64_tr_b16 v[40:41], v10 offset:24576\n" \
  "ds_read_b64_tr_b16 v[42:43], v10 offset:25600\n" \
  "ds_read_b64_tr_b16 v[44:45], v11 offset:24576\n" \
  "ds_read_b64_tr_b16 v[46:47], v11 offset:25600\n" \
  "v_mfma_f32_32x32x16_bf16 v[240:255], v[36:39], v[124:127], v[240:255]\n" \
  "s_add_u32 m0, s59, 16384\n" \
  "s_nop 0\n" \
  "buffer_load_dwordx4 v8, s[20:23], s56 offen lds\n" \
  "buffer_load_dwordx4 v7, s[20:23], s56 offen offset:1024 lds\n" \
  "s_add_u32 s56, s56, 10752\n" \
  "v_mfma_f32_32x32x16_bf16 v[192:207], v[32:35], v[120:123], v[192:207]\n" \
  "v_mfma_f32_32x32x16_bf16 v[224:239], v[36:39], v[120:123], v[224:239]\n" \
  "s_waitcnt vmcnt(18)\n" \
  "s_waitcnt lgkmcnt(0)\n" \
  "v_mfma_f32_32x32x16_bf16 v[208:223], v[40:43], v[132:135], v[208:223]\n" \
  "ds_read_b64_tr_b16 v[32:33], v10 offset:32768\n" \
  "ds_read_b64_tr_b16 v[34:35], v10 offset:33792\n" \
  "ds_read_b64_tr_b16 v[36:37], v11 offset:32768\n" \
  "ds_read_b64_tr_b16 v[38:39], v11 offset:33792\n" \
  "v_mfma_f32_32x32x16_bf16 v[240:255], v[44:47], v[132:135], v[240:255]\n" \
  "v_and_b32 v28, 0xffff0000, v28\n" \
  "v_rcp_f32 v28, v28\n" \
  "v_mfma_f32_32x32x16_bf16 v[192:207], v[40:43], v[128:131], v[192:207]\n" \
  "v_mfma_f32_32x32x16_bf16 v[224:239], v[44:47], v[128:131], v[224:239]\n" \
  "s_and_b32 s54, s48, 1\n" \
  "s_xor_b32 s60, s54, s45\n" \
  "s_and_b32 s60, s60, 1\n" \
  "s_lshl_b32 s60, s60, 11\n" \
  "s_lshl_b32 s54, s54, 13\n" \
  "s_xor_b32 s56, s54, 8192\n" \
  "s_add_u32 s54, s58, s54\n" \
  "s_add_u32 s55, s54, 4096\n" \
  "s_add_u32 s56, s58, s56\n" \
  "s_add_u32 s57, s56, 4096\n" \
  "s_mov_b32 s63, 0x100000\n" \
  "s_waitcnt vmcnt(0)\n" \
  "s_barrier\n" \
  "buffer_load_dwordx4 v[48:51], v24, s[36:39], s54 offen offset:0 sc1\n" \
  "buffer_load_dwordx4 v[52:55], v24, s[36:39], s54 offen offset:1024 sc1\n" \
  "buffer_load_dwordx4 v[56:59], v24, s[36:39], s54 offen offset:2048 sc1\n" \
  "buffer_load_dwordx4 v[60:63], v24, s[36:39], s54 offen offset:3072 sc1\n" \
  "s_waitcnt lgkmcnt(0)\n" \
  "v_mfma_f32_32x32x16_bf16 v[208:223], v[32:35], v[140:143], v[208:223]\n" \
  "ds_read_b64_tr_b16 v[40:41], v10 offset:40960\n" \
  "ds_read_b64_tr_b16 v[42:43], v10 offset:41984\n" \
  "ds_read_b64_tr_b16 v[44:45], v11 offset:40960\n" \
  "ds_read_b64_tr_b16 v[46:47], v11 offset:41984\n" \
  "v_mfma_f32_32x32x16_bf16 v[240:255], v[36:39], v[140:143], v[240:255]\n" \
  "buffer_load_dwordx4 v[64:67], v24, s[36:39], s55 offen offset:0 sc1\n" \
  "v_and_b32 v8, 7, v0\n" \
  "v_bfe_u32 v29, v0, 1, 1\n" \
  "v_xor_b32 v29, v29, v4\n" \
  "v_lshl_or_b32 v8, v29, 3, v8\n" \
  "v_lshlrev_b32 v8, 4, v8\n" \
  "v_mul_f32 v28, v28, v28\n" \
  "v_mfma_f32_32x32x16_bf16 v[192:207], v[32:35], v[136:139], v[192:207]\n" \
  "v_lshl_or_b32 v8, v3, 8, v8\n" \
  "s_lshl_b32 s53, s45, 13\n" \
  "v_add_u32 v8, s53, v8\n" \
  "v_mfma_f32_32x32x16_bf16 v[224:239], v[36:39], v[136:139], v[224:239]\n" \
  "buffer_load_dwordx4 v[68:71], v24, s[36:39], s55 offen offset:1024 sc1\n" \
  "s_waitcnt lgkmcnt(0)\n" \
  "v_mfma_f32_32x32x16_bf16 v[208:223], v[40:43], v[148:151], v[208:223]\n" \
  "v_mfma_f32_32x32x16_bf16 v[240:255], v[44:47], v[148:151], v[240:255]\n" \
  "buffer_load_dwordx4 v[72:75], v24, s[36:39], s55 offen offset:2048 sc1\n" \
  "v_xor_b32 v10, s60, v10\n" \
  "v_xor_b32 v11, s60, v11\n" \
  "v_xor_b32 v12, s60, v12\n" \
  "v_xor_b32 v13, s60, v13\n" \
  "v_mfma_f32_32x32x16_bf16 v[192:207], v[40:43], v[144:147], v[192:207]\n" \
  "v_add_u32 v27, s53, v2\n" \
  "v_xor_b32 v26, 0x2000, v27\n" \
  "v_mfma_f32_32x32x16_bf16 v[224:239], v[44:47], v[144:147], v[224:239]\n" \
  "buffer_load_dwordx4 v[76:79], v24, s[36:39], s55 offen offset:3072 sc1\n" \
    \
  "ds_read_b64_tr_b16 v[144:145], v10 offset:49152\n" \
  "ds_read_b64_tr_b16 v[146:147], v10 offset:50176\n" \
  "ds_read_b64_tr_b16 v[148:149], v11 offset:49152\n" \
  "ds_read_b64_tr_b16 v[150:151], v11 offset:50176\n" \
  "ds_read_b64_tr_b16 v[152:153], v12 offset:0\n" \
  "ds_read_b64_tr_b16 v[154:155], v12 offset:1024\n" \
  "ds_read_b64_tr_b16 v[156:157], v13 offset:0\n" \
  "ds_read_b64_tr_b16 v[158:159], v13 offset:1024\n" \
  "ds_read_b64_tr_b16 v[160:161], v12 offset:8192\n" \
  "ds_read_b64_tr_b16 v[162:163], v12 offset:9216\n" \
  "ds_read_b64_tr_b16 v[164:165], v13 offset:8192\n" \
  "ds_read_b64_tr_b16 v[166:167], v13 offset:9216\n" \
  "v_xor_b32 v184, 0x800, v10\n" \
  "v_xor_b32 v185, 0x800, v11\n" \
  "v_xor_b32 v186, 0x800, v12\n" \
  "v_xor_b32 v187, 0x800, v13\n" \
  "s_waitcnt vmcnt(4)\n" \
  "v_max3_u32 v29, v48, v49, v50\n" \
  "v_max3_u32 v30, v51, v52, v53\n" \
  "v_max3_u32 v29, v54, v55, v29\n" \
  "v_max3_u32 v30, v56, v57, v30\n" \
  "v_max3_u32 v29, v58, v59, v29\n" \
  "v_max3_u32 v30, v60, v61, v30\n" \
  "v_max3_u32 v29, v62, v63, v29\n" \
  "v_max_u32 v29, v29, v30\n" \
  "v_cmp_eq_u32 vcc, -1, v29\n" \
  "s_cbranch_vccnz .Lfz_oret0\n" \
  ".Lfz_ogo0:\n" \
  "s_waitcnt lgkmcnt(8)\n" \
  "v_mfma_f32_32x32x16_bf16 v[112:127], v[148:151], v[48:51], 0\n" \
  "ds_read_b64_tr_b16 v[168:169], v12 offset:16384\n" \
  "ds_read_b64_tr_b16 v[170:171], v12 offset:17408\n" \
  "ds_read_b64_tr_b16 v[172:173], v13 offset:16384\n" \
  "ds_read_b64_tr_b16 v[174:175], v13 offset:17408\n" \
  "v_mfma_f32_32x32x16_bf16 v[32:47], v[144:147], v[48:51], 0\n" \
  "buffer_load_dwordx4 v[80:83], v24, s[36:39], s56 offen offset:0 sc1\n" \
  "ds_write_b128 v27, v[240:243] offset:4096\n" \
  "v_lshlrev_b32 v128, 16, v16\n" \
  "v_and_b32 v129, 0xffff0000, v16\n" \
  "v_lshlrev_b32 v130, 16, v17\n" \
  "v_and_b32 v131, 0xffff0000, v17\n" \
  "s_waitcnt lgkmcnt(9)\n" \
  "v_mfma_f32_32x32x16_bf16 v[112:127], v[156:159], v[52:55], v[112:127]\n" \
  "ds_read_b64_tr_b16 v[176:177], v12 offset:24576\n" \
  "ds_read_b64_tr_b16 v[178:179], v12 offset:25600\n" \
  "ds_read_b64_tr_b16 v[180:181], v13 offset:24576\n" \
  "ds_read_b64_tr_b16 v[182:183], v13 offset:25600\n" \
  "v_mfma_f32_32x32x16_bf16 v[32:47], v[152:155], v[52:55], v[32:47]\n" \
  "buffer_load_dwordx4 v[84:87], v24, s[36:39], s56 offen offset:1024 sc1\n" \
  "ds_write_b128 v27, v[244:247] offset:5120\n" \
  "v_lshlrev_b32 v132, 16, v18\n" \
  "v_and_b32 v133, 0xffff0000, v18\n" \
  "v_lshlrev_b32 v134, 16, v19\n" \
  "v_and_b32 v135, 0xffff0000, v19\n" \
  "s_waitcnt lgkmcnt(10)\n" \
  "v_mfma_f32_32x32x16_bf16 v[112:127], v[164:167], v[56:59], v[112:127]\n" \
  "ds_read_b64_tr_b16 v[144:145], v10 offset:0\n" \
  "ds_read_b64_tr_b16 v[146:147], v10 offset:1024\n" \
  "ds_read_b64_tr_b16 v[148:149], v11 offset:0\n" \
  "ds_read_b64_tr_b16 v[150:151], v11 offset:1024\n" \
  "v_mfma_f32_32x32x16_bf16 v[32:47], v[160:163], v[56:59], v[32:47]\n" \
  "buffer_load_dwordx4 v[88:91], v24, s[36:39], s56 offen offset:2048 sc1\n" \
  "ds_write_b128 v27, v[248:251] offset:6144\n" \
  "s_waitcnt vmcnt(3)\n" \
  "v_max3_u32 v29, v64, v65, v66\n" \
  "v_max3_u32 v30, v67, v68, v69\n" \
  "v_max3_u32 v29, v70, v71, v29\n" \
  "v_max3_u32 v30, v72, v73, v30\n" \
  "s_waitcnt lgkmcnt(11)\n" \
  "v_mfma_f32_32x32x16_bf16 v[112:127], v[172:175], v[60:63], v[112:127]\n" \
  "ds_read_b64_tr_b16 v[152:153], v10 offset:8192\n" \
  "ds_read_b64_tr_b16 v[154:155], v10 offset:9216\n" \
  "ds_read_b64_tr_b16 v[156:157], v11 offset:8192\n" \
  "ds_read_b64_tr_b16 v[158:159], v11 offset:9216\n" \
  "v_mfma_f32_32x32x16_bf16 v[32:47], v[168:171], v[60:63], v[32:47]\n" \
  "buffer_load_dwordx4 v[92:95], v24, s[36:39], s56 offen offset:3072 sc1\n" \
  "ds_write_b128 v27, v[252:255] offset:7168\n" \
  "v_max3_u32 v29, v74, v75, v29\n" \
  "v_max3_u32 v30, v76, v77, v30\n" \
  "v_max3_u32 v29, v78, v79, v29\n" \
  "v_max_u32 v29, v29, v30\n" \
  "v_cmp_eq_u32 vcc, -1, v29\n" \
  "s_cbranch_vccnz .Lfz_oret1\n" \
  ".Lfz_ogo1:\n" \
  "s_waitcnt lgkmcnt(11)\n" \
  "v_mfma_f32_32x32x16_bf16 v[112:127], v[180:183], v[64:67], v[112:127]\n" \
  "ds_read_b64_tr_b16 v[160:161], v10 offset:16384\n" \
  "ds_read_b64_tr_b16 v[162:163], v10 offset:17408\n" \
  "ds_read_b64_tr_b16 v[164:165], v11 offset:16384\n" \
  "ds_read_b64_tr_b16 v[166:167], v11 offset:17408\n" \
  "v_mfma_f32_32x32x16_bf16 v[32:47], v[176:179], v[64:67], v[32:47]\n" \
  "buffer_load_dwordx4 v[96:99], v24, s[36:39], s57 offen offset:0 sc1\n" \
  "ds_write_b128 v27, v[208:211] offset:0\n" \
  "v_xor_b32 v136, 0, v8\n" \
  "v_xor_b32 v137, 16, v8\n" \
  "v_xor_b32 v138, 32, v8\n" \
  "v_xor_b32 v139, 48, v8\n" \
  "s_waitcnt lgkmcnt(11)\n" \
  "v_mfma_f32_32x32x16_bf16 v[112:127], v[148:151], v[68:71], v[112:127]\n" \
  "ds_read_b64_tr_b16 v[168:169], v184 offset:49152\n" \
  "ds_read_b64_tr_b16 v[170:171], v184 offset:50176\n" \
  "ds_read_b64_tr_b16 v[172:173], v185 offset:49152\n" \
  "ds_read_b64_tr_b16 v[174:175], v185 offset:50176\n" \
  "v_mfma_f32_32x32x16_bf16 v[32:47], v[144:147], v[68:71], v[32:47]\n" \
  "buffer_load_dwordx4 v[100:103], v24, s[36:39], s57 offen offset:1024 sc1\n" \
  "ds_write_b128 v27, v[212:215] offset:1024\n" \
  "v_xor_b32 v140, 64, v8\n" \
  "v_xor_b32 v141, 80, v8\n" \
  "v_xor_b32 v142, 96, v8\n" \
  "v_xor_b32 v143, 112, v8\n" \
  "s_waitcnt lgkmcnt(11)\n" \
  "v_mfma_f32_32x32x16_bf16 v[112:127], v[156:159], v[72:75], v[112:127]\n" \
  "ds_read_b64_tr_b16 v[176:177], v186 offset:0\n" \
  "ds_read_b64_tr_b16 v[178:179], v186 offset:1024\n" \
  "ds_read_b64_tr_b16 v[180:181], v187 offset:0\n" \
  "ds_read_b64_tr_b16 v[182:183], v187 offset:1024\n" \
  "v_mfma_f32_32x32x16_bf16 v[32:47], v[152:155], v[72:75], v[32:47]\n" \
  "buffer_load_dwordx4 v[104:107], v24, s[36:39], s57 offen offset:2048 sc1\n" \
  "ds_write_b128 v27, v[216:219] offset:2048\n" \
  "s_waitcnt vmcnt(3)\n" \
  "v_max3_u32 v29, v80, v81, v82\n" \
  "v_max3_u32 v30, v83, v84, v85\n" \
  "v_max3_u32 v29, v86, v87, v29\n" \
  "v_max3_u32 v30, v88, v89, v30\n" \
  "s_waitcnt lgkmcnt(11)\n" \
  "v_mfma_f32_32x32x16_bf16 v[112:127], v[164:167], v[76:79], v[112:127]\n" \
  "ds_read_b64_tr_b16 v[144:145], v186 offset:8192\n" \
  "ds_read_b64_tr_b16 v[146:147], v186 offset:9216\n" \
  "ds_read_b64_tr_b16 v[148:149], v187 offset:8192\n" \
  "ds_read_b64_tr_b16 v[150:151], v187 offset:9216\n" \
  "v_mfma_f32_32x32x16_bf16 v[32:47], v[160:163], v[76:79], v[32:47]\n" \
  "buffer_load_dwordx4 v[108:111], v24, s[36:39], s57 offen offset:3072 sc1\n" \
  "ds_write_b128 v27, v[220:223] offset:3072\n" \
  "v_max3_u32 v29, v90, v91, v29\n" \
  "v_max3_u32 v30, v92, v93, v30\n" \
  "v_max3_u32 v29, v94, v95, v29\n" \
  "v_max_u32 v29, v29, v30\n" \
  "v_cmp_eq_u32 vcc, -1, v29\n" \
  "s_cbranch_vccnz .Lfz_oret2\n" \
  ".Lfz_ogo2:\n" \
  "s_waitcnt lgkmcnt(11)\n" \
  "v_mfma_f32_32x32x16_bf16 v[112:127], v[172:175], v[80:83], v[112:127]\n" \
  "ds_read_b64_tr_b16 v[152:153], v186 offset:16384\n" \
  "ds_read_b64_tr_b16 v[154:155], v186 offset:17408\n" \
  "ds_read_b64_tr_b16 v[156:157], v187 offset:16384\n" \
  "ds_read_b64_tr_b16 v[158:159], v187 offset:17408\n" \
  "v_mfma_f32_32x32x16_bf16 v[32:47], v[168:171], v[80:83], v[32:47]\n" \
  "s_waitcnt lgkmcnt(4)\n" \
  "s_barrier\n" \
  "ds_read_b128 v[240:243], v26 offset:4096\n" \
  "ds_read_b128 v[244:247], v26 offset:5120\n" \
  "v_mfma_f32_32x32x16_bf16 v[112:127], v[180:183], v[84:87], v[112:127]\n" \
  "ds_read_b64_tr_b16 v[160:161], v186 offset:24576\n" \
  "ds_read_b64_tr_b16 v[162:163], v186 offset:25600\n" \
  "ds_read_b64_tr_b16 v[164:165], v187 offset:24576\n" \
  "ds_read_b64_tr_b16 v[166:167], v187 offset:25600\n" \
  "v_mfma_f32_32x32x16_bf16 v[32:47], v[176:179], v[84:87], v[32:47]\n" \
  "ds_read_b128 v[248:251], v26 offset:6144\n" \
  "ds_read_b128 v[252:255], v26 offset:7168\n" \
  "v_mfma_f32_32x32x16_bf16 v[112:127], v[148:151], v[88:91], v[112:127]\n" \
  "ds_read_b64_tr_b16 v[168:169], v184 offset:0\n" \
  "ds_read_b64_tr_b16 v[170:171], v184 offset:1024\n" \
  "ds_read_b64_tr_b16 v[172:173], v185 offset:0\n" \
  "ds_read_b64_tr_b16 v[174:175], v185 offset:1024\n" \
  "v_mfma_f32_32x32x16_bf16 v[32:47], v[144:147], v[88:91], v[32:47]\n" \
  "ds_read_b128 v[208:211], v26 offset:0\n" \
  "ds_read_b128 v[212:215], v26 offset:1024\n" \
  "s_waitcnt vmcnt(0)\n" \
  "v_max3_u32 v29, v96, v97, v98\n" \
  "v_max3_u32 v30, v99, v100, v101\n" \
  "v_max3_u32 v29, v102, v103, v29\n" \
  "v_max3_u32 v30, v104, v105, v30\n" \
  "s_waitcnt lgkmcnt(14)\n" \
  "v_mfma_f32_32x32x16_bf16 v[112:127], v[156:159], v[92:95], v[112:127]\n" \
  "ds_read_b64_tr_b16 v[176:177], v184 offset:8192\n" \
  "ds_read_b64_tr_b16 v[178:179], v184 offset:9216\n" \
  "ds_read_b64_tr_b16 v[180:181], v185 offset:8192\n" \
  "ds_read_b64_tr_b16 v[182:183], v185 offset:9216\n" \
  "v_mfma_f32_32x32x16_bf16 v[32:47], v[152:155], v[92:95], v[32:47]\n" \
  "ds_read_b128 v[216:219], v26 offset:2048\n" \
  "ds_read_b128 v[220:223], v26 offset:3072\n" \
  "v_max3_u32 v29, v106, v107, v29\n" \
  "v_max3_u32 v30, v108, v109, v30\n" \
  "v_max3_u32 v29, v110, v111, v29\n" \
  "v_max_u32 v29, v29, v30\n" \
  "v_cmp_eq_u32 vcc, -1, v29\n" \
  "s_cbranch_vccnz .Lfz_oret3\n" \
  ".Lfz_ogo3:\n" \
  "s_waitcnt lgkmcnt(14)\n" \
  "v_mfma_f32_32x32x16_bf16 v[112:127], v[164:167], v[96:99], v[112:127]\n" \
  "ds_read_b64_tr_b16 v[144:145], v184 offset:16384\n" \
  "ds_read_b64_tr_b16 v[146:147], v184 offset:17408\n" \
  "ds_read_b64_tr_b16 v[148:149], v185 offset:16384\n" \
  "ds_read_b64_tr_b16 v[150:151], v185 offset:17408\n" \
  "v_add_f32 v224, v224, v240\n" \
  "v_add_f32 v225, v225, v241\n" \
  "v_add_f32 v226, v226, v242\n" \
  "v_add_f32 v227, v227, v243\n" \
  "s_waitcnt lgkmcnt(12)\n" \
  "v_mfma_f32_32x32x16_bf16 v[112:127], v[172:175], v[100:103], v[112:127]\n" \
  "v_add_f32 v228, v228, v244\n" \
  "v_add_f32 v229, v229, v245\n" \
  "v_add_f32 v230, v230, v246\n" \
  "v_add_f32 v231, v231, v247\n" \
  "s_waitcnt lgkmcnt(6)\n" \
  "v_mfma_f32_32x32x16_bf16 v[112:127], v[180:183], v[104:107], v[112:127]\n" \
  "v_add_f32 v232, v232, v248\n" \
  "v_add_f32 v233, v233, v249\n" \
  "v_add_f32 v234, v234, v250\n" \
  "v_add_f32 v235, v235, v251\n" \
  "s_waitcnt lgkmcnt(0)\n" \
  "v_mfma_f32_32x32x16_bf16 v[112:127], v[148:151], v[108:111], v[112:127]\n" \
  "v_add_f32 v236, v236, v252\n" \
  "v_add_f32 v237, v237, v253\n" \
  "v_add_f32 v238, v238, v254\n" \
  "v_add_f32 v239, v239, v255\n" \
  "v_mfma_f32_32x32x16_bf16 v[32:47], v[160:163], v[96:99], v[32:47]\n" \
  "v_add_f32 v192, v192, v208\n" \
  "v_add_f32 v193, v193, v209\n" \
  "v_add_f32 v194, v194, v210\n" \
  "v_add_f32 v195, v195, v211\n" \
  "v_add_f32 v196, v196, v212\n" \
  "v_add_f32 v197, v197, v213\n" \
  "v_add_f32 v198, v198, v214\n" \
  "v_add_f32 v199, v199, v215\n" \
  "v_mfma_f32_32x32x16_bf16 v[32:47], v[168:171], v[100:103], v[32:47]\n" \
  "v_add_f32 v200, v200, v216\n" \
  "v_add_f32 v201, v201, v217\n" \
  "v_add_f32 v202, v202, v218\n" \
  "v_add_f32 v203, v203, v219\n" \
  "v_add_f32 v204, v204, v220\n" \
  "v_add_f32 v205, v205, v221\n" \
  "v_add_f32 v206, v206, v222\n" \
  "v_add_f32 v207, v207, v223\n" \
  "v_mfma_f32_32x32x16_bf16 v[32:47], v[176:179], v[104:107], v[32:47]\n" \
  "v_fmac_f32 v112, v28, v224\n" \
  "v_fmac_f32 v113, v28, v225\n" \
  "v_fmac_f32 v114, v28, v226\n" \
  "v_fmac_f32 v115, v28, v227\n" \
  "ds_write_b128 v140, v[112:115] offset:32768\n" \
  "v_mfma_f32_32x32x16_bf16 v[32:47], v[144:147], v[108:111], v[32:47]\n" \
  "v_fmac_f32 v116, v28, v228\n" \
  "v_fmac_f32 v117, v28, v229\n" \
  "v_fmac_f32 v118, v28, v230\n" \
  "v_fmac_f32 v119, v28, v231\n" \
  "ds_write_b128 v141, v[116:119] offset:32768\n" \
  "v_fmac_f32 v120, v28, v232\n" \
  "v_fmac_f32 v121, v28, v233\n" \
  "v_fmac_f32 v122, v28, v234\n" \
  "v_fmac_f32 v123, v28, v235\n" \
  "ds_write_b128 v142, v[120:123] offset:32768\n" \
  "v_fmac_f32 v124, v28, v236\n" \
  "v_fmac_f32 v125, v28, v237\n" \
  "v_fmac_f32 v126, v28, v238\n" \
  "v_fmac_f32 v127, v28, v239\n" \
  "ds_write_b128 v143, v[124:127] offset:32768\n" \
  "v_fmac_f32 v32, v28, v192\n" \
  "v_fmac_f32 v33, v28, v193\n" \
  "v_fmac_f32 v34, v28, v194\n" \
  "v_fmac_f32 v35, v28, v195\n" \
  "ds_write_b128 v136, v[32:35] offset:32768\n" \
  "v_fmac_f32 v36, v28, v196\n" \
  "v_fmac_f32 v37, v28, v197\n" \
  "v_fmac_f32 v38, v28, v198\n" \
  "v_fmac_f32 v39, v28, v199\n" \
  "ds_write_b128 v137, v[36:39] offset:32768\n" \
  "v_fmac_f32 v40, v28, v200\n" \
  "v_fmac_f32 v41, v28, v201\n" \
  "v_fmac_f32 v42, v28, v202\n" \
  "v_fmac_f32 v43, v28, v203\n" \
  "ds_write_b128 v138, v[40:43] offset:32768\n" \
  "v_fmac_f32 v44, v28, v204\n" \
  "v_fmac_f32 v45, v28, v205\n" \
  "v_fmac_f32 v46, v28, v206\n" \
  "v_fmac_f32 v47, v28, v207\n" \
  "ds_write_b128 v139, v[44:47] offset:32768\n" \
  "s_waitcnt lgkmcnt(0)\n" \
  "s_barrier\n" \
  "ds_read_b128 v[192:195], v20 offset:32768\n" \
  "ds_read_b128 v[196:199], v21 offset:32768\n" \
  "ds_read_b128 v[200:203], v20 offset:49152\n" \
  "ds_read_b128 v[204:207], v21 offset:49152\n" \
  "s_waitcnt lgkmcnt(2)\n" \
  "v_add_f32 v128, v128, v192\n" \
  "v_add_f32 v129, v129, v193\n" \
  "v_add_f32 v130, v130, v194\n" \
  "v_add_f32 v131, v131, v195\n" \
  "v_add_f32 v132, v132, v196\n" \
  "v_add_f32 v133, v133, v197\n" \
  "v_add_f32 v134, v134, v198\n" \
  "v_add_f32 v135, v135, v199\n" \
  "s_waitcnt lgkmcnt(0)\n" \
  "v_add_f32 v128, v128, v200\n" \
  "v_add_f32 v129, v129, v201\n" \
  "v_add_f32 v130, v130, v202\n" \
  "v_add_f32 v131, v131, v203\n" \
  "v_add_f32 v132, v132, v204\n" \
  "v_add_f32 v133, v133, v205\n" \
  "v_add_f32 v134, v134, v206\n" \
  "v_add_f32 v135, v135, v207\n" \
  "v_cvt_pk_bf16_f32 v192, v128, v129\n" \
  "v_cvt_pk_bf16_f32 v193, v130, v131\n" \
  "v_cvt_pk_bf16_f32 v194, v132, v133\n" \
  "v_cvt_pk_bf16_f32 v195, v134, v135\n" \
  "buffer_store_dwordx4 v[192:195], v14, s[40:43], s61 offen sc0 sc1\n" \
  "s_endpgm\n" \
  ".Lfz_SG:\n" \
    \
  "v_bfe_u32 v16, v0, 3, 3\n" \
  "v_mul_u32_u24 v16, 896, v16\n" \
  "v_bfe_u32 v23, v0, 4, 2\n" \
  "v_and_b32 v24, 7, v0\n" \
  "v_xor_b32 v24, v24, v23\n" \
  "v_lshl_add_u32 v16, v24, 4, v16\n" \
  "s_lshl2_add_u32 s55, s46, s45\n" \
  "s_mul_i32 s55, s55, 28672\n" \
  "s_lshl_b32 s59, s45, 12\n" \
  "v_xor_b32 v17, 64, v16\n" \
  "v_add_u32 v17, 6144, v17\n" \
  "v_add_u32 v18, 12288, v16\n" \
  "v_add_u32 v19, 12288, v17\n" \
  "v_and_b32 v1, 63, v0\n" \
  "v_lshlrev_b32 v2, 4, v1\n" \
  "s_mul_i32 s53, s45, 7168\n" \
  "s_add_u32 m0, s53, 114688\n" \
  "s_cmp_eq_u32 s47, 0\n" \
  "s_waitcnt lgkmcnt(0)\n" \
  "s_cselect_b64 s[12:13], s[12:13], s[16:17]\n" \
  "buffer_load_dwordx4 v2, s[12:15], s53 offen offset:0 lds\n" \
  "buffer_load_dwordx4 v2, s[12:15], s53 offen offset:1024 lds\n" \
  "buffer_load_dwordx4 v2, s[12:15], s53 offen offset:2048 lds\n" \
  "buffer_load_dwordx4 v2, s[12:15], s53 offen offset:3072 lds\n" \
  "s_add_u32 m0, m0, 4096\n" \
  "s_add_u32 s53, s53, 4096\n" \
  "buffer_load_dwordx4 v2, s[12:15], s53 offen offset:0 lds\n" \
  "buffer_load_dwordx4 v2, s[12:15], s53 offen offset:1024 lds\n" \
  "buffer_load_dwordx4 v2, s[12:15], s53 offen offset:2048 lds\n" \
  "s_add_u32 m0, s59, 0\n" \
  "s_add_u32 s58, s55, 0\n" \
  "buffer_load_dwordx4 v16, s[4:7], s58 offen offset:0 lds\n" \
  "buffer_load_dwordx4 v17, s[4:7], s58 offen offset:1024 lds\n" \
  "buffer_load_dwordx4 v18, s[4:7], s58 offen offset:2048 lds\n" \
  "buffer_load_dwordx4 v19, s[4:7], s58 offen offset:3072 lds\n" \
  "s_add_u32 m0, s59, 16384\n" \
  "s_add_u32 s58, s55, 128\n" \
  "buffer_load_dwordx4 v16, s[4:7], s58 offen offset:0 lds\n" \
  "buffer_load_dwordx4 v17, s[4:7], s58 offen offset:1024 lds\n" \
  "buffer_load_dwordx4 v18, s[4:7], s58 offen offset:2048 lds\n" \
  "buffer_load_dwordx4 v19, s[4:7], s58 offen offset:3072 lds\n" \
  "s_waitcnt vmcnt(4)\n" \
  "s_barrier\n" \
  "s_add_u32 m0, s59, 32768\n" \
  "s_add_u32 s58, s55, 256\n" \
  "buffer_load_dwordx4 v16, s[4:7], s58 offen offset:0 lds\n" \
  "buffer_load_dwordx4 v17, s[4:7], s58 offen offset:1024 lds\n" \
  "buffer_load_dwordx4 v18, s[4:7], s58 offen offset:2048 lds\n" \
  "buffer_load_dwordx4 v19, s[4:7], s58 offen offset:3072 lds\n" \
  "v_bfe_u32 v20, v0, 2, 4\n" \
  "v_mul_u32_u24 v20, 896, v20\n" \
  "v_and_b32 v24, 3, v0\n" \
  "v_lshl_add_u32 v20, v24, 4, v20\n" \
  "s_mul_i32 s57, s49, 56\n" \
  "s_and_b32 s57, s57, 0xfffffff0\n" \
  "s_mul_i32 s53, s45, 100352\n" \
  "s_add_u32 s57, s57, s53\n" \
  "s_mul_i32 s53, s45, 7168\n" \
  "s_add_u32 s53, s53, 114688\n" \
  "v_add_u32 v21, s53, v2\n" \
  "s_add_u32 m0, s59, 49152\n" \
  "s_add_u32 s58, s55, 384\n" \
  "buffer_load_dwordx4 v16, s[4:7], s58 offen offset:0 lds\n" \
  "buffer_load_dwordx4 v17, s[4:7], s58 offen offset:1024 lds\n" \
  "buffer_load_dwordx4 v18, s[4:7], s58 offen offset:2048 lds\n" \
  "buffer_load_dwordx4 v19, s[4:7], s58 offen offset:3072 lds\n" \
  "s_waitcnt vmcnt(8)\n" \
  "s_barrier\n" \
  "s_add_u32 m0, s59, 65536\n" \
  "s_add_u32 s58, s55, 512\n" \
  "buffer_load_dwordx4 v16, s[4:7], s58 offen offset:0 lds\n" \
  "buffer_load_dwordx4 v17, s[4:7], s58 offen offset:1024 lds\n" \
  "buffer_load_dwordx4 v18, s[4:7], s58 offen offset:2048 lds\n" \
  "buffer_load_dwordx4 v19, s[4:7], s58 offen offset:3072 lds\n" \
  "s_waitcnt vmcnt(8)\n" \
  "s_barrier\n" \
  "s_add_u32 m0, s59, 81920\n" \
  "s_add_u32 s58, s55, 640\n" \
  "buffer_load_dwordx4 v16, s[4:7], s58 offen offset:0 lds\n" \
  "buffer_load_dwordx4 v17, s[4:7], s58 offen offset:1024 lds\n" \
  "buffer_load_dwordx4 v18, s[4:7], s58 offen offset:2048 lds\n" \
  "buffer_load_dwordx4 v19, s[4:7], s58 offen offset:3072 lds\n" \
  "s_waitcnt vmcnt(8)\n" \
  "s_barrier\n" \
  "s_add_u32 m0, s59, 98304\n" \
  "s_add_u32 s58, s55, 768\n" \
  "buffer_load_dwordx4 v16, s[4:7], s58 offen offset:0 lds\n" \
  "buffer_load_dwordx4 v17, s[4:7], s58 offen offset:1024 lds\n" \
  "buffer_load_dwordx4 v18, s[4:7], s58 offen offset:2048 lds\n" \
  "buffer_load_dwordx4 v19, s[4:7], s58 offen offset:3072 lds\n" \
  "s_waitcnt vmcnt(8)\n" \
  "s_barrier\n" \
  "s_waitcnt vmcnt(4)\n" \
  "s_barrier\n" \
  "buffer_load_dwordx4 v[32:35], v20, s[8:11], s57 offen\n" \
  "s_add_u32 s57, s57, 14336\n" \
  "s_waitcnt vmcnt(1)\n" \
  "s_barrier\n" \
  "buffer_load_dwordx4 v[36:39], v20, s[8:11], s57 offen\n" \
  "s_add_u32 s57, s57, 14336\n" \
  "s_barrier\n" \
  "buffer_load_dwordx4 v[40:43], v20, s[8:11], s57 offen\n" \
  "s_add_u32 s57, s57, 14336\n" \
  "s_barrier\n" \
  "buffer_load_dwordx4 v[44:47], v20, s[8:11], s57 offen\n" \
  "s_add_u32 s57, s57, 14336\n" \
  "buffer_load_dwordx4 v[48:51], v20, s[8:11], s57 offen\n" \
  "s_add_u32 s57, s57, 14336\n" \
  "buffer_load_dwordx4 v[52:55], v20, s[8:11], s57 offen\n" \
  "s_add_u32 s57, s57, 14336\n" \
  "buffer_load_dwordx4 v[56:59], v20, s[8:11], s57 offen\n" \
  "s_add_u32 s57, s57, 14336\n" \
  "s_waitcnt vmcnt(6)\n" \
  "ds_write_b128 v21, v[32:35] offset:0\n" \
  "s_waitcnt vmcnt(5)\n" \
  "ds_write_b128 v21, v[36:39] offset:1024\n" \
  "s_waitcnt vmcnt(4)\n" \
  "ds_write_b128 v21, v[40:43] offset:2048\n" \
  "s_waitcnt vmcnt(3)\n" \
  "ds_write_b128 v21, v[44:47] offset:3072\n" \
  "s_waitcnt vmcnt(2)\n" \
  "ds_write_b128 v21, v[48:51] offset:4096\n" \
  "s_waitcnt vmcnt(1)\n" \
  "ds_write_b128 v21, v[52:55] offset:5120\n" \
  "s_waitcnt vmcnt(0)\n" \
  "ds_write_b128 v21, v[56:59] offset:6144\n" \
  "s_waitcnt lgkmcnt(0)\n" \
  "s_barrier\n" \
    \
  "s_lshl2_add_u32 s53, s2, s45\n" \
  "v_min_u32 v22, 48, v1\n" \
  "v_lshlrev_b32 v22, 7, v22\n" \
  "v_min_u32 v23, 24, v1\n" \
  "v_lshlrev_b32 v23, 7, v23\n" \
  "s_mul_i32 s54, s53, 12544\n" \
  "buffer_load_dword v64, v22, s[32:35], s54 offen\n" \
  "s_add_u32 s54, s54, 6272\n" \
  "buffer_load_dword v65, v22, s[32:35], s54 offen\n" \
  "s_mul_i32 s54, s53, 3136\n" \
  "buffer_load_dword v66, v23, s[20:23], s54 offen\n" \
  "s_barrier\n" \
  "s_barrier\n" \
  "s_endpgm\n" \
  ".Lfz_SA:\n" \
    \
  "v_and_b32 v1, 63, v0\n" \
  "v_lshlrev_b32 v2, 4, v1\n" \
  "v_and_b32 v3, 31, v0\n" \
  "v_bfe_u32 v4, v0, 5, 1\n" \
  "v_bfe_u32 v5, v0, 2, 2\n" \
  "v_bfe_u32 v7, v0, 4, 1\n" \
  "v_and_b32 v6, 3, v0\n" \
  "v_lshl_add_u32 v6, v7, 2, v6\n" \
  "v_bfe_u32 v7, v0, 1, 3\n" \
  "v_xor_b32 v7, v7, v4\n" \
  "v_lshlrev_b32 v7, 4, v7\n" \
  "v_lshl_add_u32 v7, v3, 7, v7\n" \
  "v_mov_b64 v[8:9], 0\n" \
  "v_mov_b32 v10, 159744\n" \
  "ds_write_b64 v10, v[8:9]\n" \
  "s_waitcnt lgkmcnt(0)\n" \
  "v_mov_b32 v244, 9981952\n" \
  "buffer_load_dword v244, v244, s[36:39], 0 offen sc1\n" \
  "s_lshr_b32 s53, s2, 3\n" \
  "s_lshl2_add_u32 s53, s53, s45\n" \
  "s_mul_i32 s54, s53, 100352\n" \
  "s_load_dword s84, s[32:33], s54\n" \
  "s_add_u32 s54, s54, 50176\n" \
  "s_load_dword s85, s[32:33], s54\n" \
  "s_mul_i32 s54, s53, 25088\n" \
  "s_load_dword s86, s[20:21], s54\n" \
  "s_and_b32 s53, s49, 1\n" \
  "v_add_u32 v18, s53, v6\n" \
  "v_and_b32 v18, 7, v18\n" \
  "v_lshl_add_u32 v17, v4, 3, v5\n" \
  "v_lshlrev_b32 v17, 6, v17\n" \
  "v_lshl_add_u32 v17, v18, 3, v17\n" \
  "v_add_u32 v17, 114688, v17\n" \
  "s_lshl_b32 s53, s45, 12\n" \
  "v_add_u32 v18, s53, v7\n" \
  "v_xor_b32 v19, 32, v18\n" \
  "v_xor_b32 v20, 64, v18\n" \
  "v_xor_b32 v21, 0x60, v18\n" \
  "s_add_u32 s53, s45, 1\n" \
  "s_and_b32 s53, s53, 3\n" \
  "s_mul_i32 s54, s53, 12288\n" \
  "s_lshl_b32 s53, s53, 11\n" \
  "s_add_u32 s53, s53, 61440\n" \
  "v_add_u32 v26, s53, v2\n" \
  "s_add_u32 s54, s54, 0\n" \
  "v_add_u32 v12, s54, v2\n" \
  "s_add_u32 s53, s45, 2\n" \
  "s_and_b32 s53, s53, 3\n" \
  "s_mul_i32 s54, s53, 12288\n" \
  "s_lshl_b32 s53, s53, 11\n" \
  "s_add_u32 s53, s53, 61440\n" \
  "v_add_u32 v27, s53, v2\n" \
  "s_add_u32 s54, s54, 4096\n" \
  "v_add_u32 v13, s54, v2\n" \
  "s_add_u32 s53, s45, 3\n" \
  "s_and_b32 s53, s53, 3\n" \
  "s_mul_i32 s54, s53, 12288\n" \
  "s_lshl_b32 s53, s53, 11\n" \
  "s_add_u32 s53, s53, 61440\n" \
  "v_add_u32 v28, s53, v2\n" \
  "s_add_u32 s54, s54, 8192\n" \
  "v_add_u32 v14, s54, v2\n" \
  "s_add_u32 s53, s45, 4\n" \
  "s_and_b32 s53, s53, 3\n" \
  "s_mul_i32 s54, s53, 12288\n" \
  "s_lshl_b32 s53, s53, 11\n" \
  "s_add_u32 s53, s53, 61440\n" \
  "v_add_u32 v29, s53, v2\n" \
  "s_add_u32 s54, s54, 0\n" \
  "v_add_u32 v15, s54, v2\n" \
  "v_mov_b32 v8, 159744\n" \
  "v_mov_b32 v30, 0xdddddddd\n" \
  "s_lshl_b32 s53, s45, 10\n" \
  "s_add_u32 s53, s53, 53248\n" \
  "v_add_u32 v9, s53, v2\n" \
  "v_and_b32 v11, 7, v0\n" \
  "s_lshl_b32 s53, s45, 10\n" \
  "s_add_u32 s53, s53, 9945088\n" \
  "v_lshl_add_u32 v11, v11, 7, s53\n" \
  "s_lshl_b32 s53, s45, 16\n" \
  "s_add_u32 s53, s53, 9437184\n" \
  "v_add_u32 v31, s53, v2\n" \
  "v_lshlrev_b32 v10, 3, v3\n" \
  "v_add_u32 v10, 768, v10\n" \
  "v_mul_u32_u24 v10, v4, v10\n" \
  "v_sub_u32 v10, v31, v10\n" \
  "v_lshlrev_b32 v16, 6, v4\n" \
  "v_sub_u32 v16, v31, v16\n" \
  "s_lshr_b32 s61, s2, 4\n" \
  "s_lshl_b32 s61, s61, 12\n" \
  "v_cmp_eq_u32 vcc, 28, v3\n" \
  "v_mov_b32 v248, 0x3f803f80\n" \
  "s_nop 0\n" \
  "v_cndmask_b32 v248, 0, v248, vcc\n" \
  "v_mov_b32 v249, v248\n" \
  "v_mov_b64 v[250:251], v[248:249]\n" \
  "v_mov_b64 v[252:253], v[248:249]\n" \
  "v_mov_b64 v[254:255], v[248:249]\n" \
  "v_mov_b64 v[200:201], v[248:249]\n" \
  "v_mov_b64 v[202:203], v[248:249]\n" \
  "v_mov_b64 v[204:205], v[248:249]\n" \
  "v_mov_b64 v[206:207], v[248:249]\n" \
  "v_mov_b64 v[216:217], v[248:249]\n" \
  "v_mov_b64 v[218:219], v[248:249]\n" \
  "v_mov_b64 v[220:221], v[248:249]\n" \
  "v_mov_b64 v[222:223], v[248:249]\n" \
  "v_mov_b64 v[232:233], v[248:249]\n" \
  "v_mov_b64 v[234:235], v[248:249]\n" \
  "v_mov_b64 v[236:237], v[248:249]\n" \
  "v_mov_b64 v[238:239], v[248:249]\n" \
  "v_mov_b32 v196, 0x3f803f80\n" \
  "v_mov_b32 v197, v196\n" \
  "v_mov_b64 v[198:199], v[196:197]\n" \
  "v_mov_b64 v[128:129], 0\n" \
  "v_mov_b64 v[130:131], 0\n" \
  "v_mov_b64 v[132:133], 0\n" \
  "v_mov_b64 v[134:135], 0\n" \
  "v_mov_b64 v[136:137], 0\n" \
  "v_mov_b64 v[138:139], 0\n" \
  "v_mov_b64 v[140:141], 0\n" \
  "v_mov_b64 v[142:143], 0\n" \
  "v_mov_b64 v[144:145], 0\n" \
  "v_mov_b64 v[146:147], 0\n" \
  "v_mov_b64 v[148:149], 0\n" \
  "v_mov_b64 v[150:151], 0\n" \
  "v_mov_b64 v[152:153], 0\n" \
  "v_mov_b64 v[154:155], 0\n" \
  "v_mov_b64 v[156:157], 0\n" \
  "v_mov_b64 v[158:159], 0\n" \
  "v_mov_b64 v[160:161], 0\n" \
  "v_mov_b64 v[162:163], 0\n" \
  "v_mov_b64 v[164:165], 0\n" \
  "v_mov_b64 v[166:167], 0\n" \
  "v_mov_b64 v[168:169], 0\n" \
  "v_mov_b64 v[170:171], 0\n" \
  "v_lshl_add_u32 v22, v4, 3, v5\n" \
  "v_mul_u32_u24 v22, 56, v22\n" \
  "v_lshl_add_u32 v22, v6, 3, v22\n" \
  "v_add_u32 v22, 114688, v22\n" \
  "s_mov_b32 s56, 0x3f803f80\n" \
  "s_setprio 1\n" \
  "s_waitcnt vmcnt(0)\n" \
  "v_readfirstlane_b32 s51, v244\n" \
  "s_nop 0\n" \
  "s_add_u32 s51, s51, 1\n" \
  "s_mov_b32 s54, 3\n" \
  "s_barrier\n" \
  "ds_read_b64_tr_b16 v[32:33], v22 offset:0\n" \
  "ds_read_b64_tr_b16 v[34:35], v22 offset:224\n" \
  "ds_read_b128 v[36:39], v18\n" \
  "ds_read_b64_tr_b16 v[40:41], v22 offset:896\n" \
  "ds_read_b64_tr_b16 v[42:43], v22 offset:1120\n" \
  "ds_read_b128 v[44:47], v19\n" \
  "ds_read_b64_tr_b16 v[48:49], v22 offset:1792\n" \
  "ds_read_b64_tr_b16 v[50:51], v22 offset:2016\n" \
  "ds_read_b128 v[52:55], v20\n" \
  "ds_read_b64_tr_b16 v[56:57], v22 offset:2688\n" \
  "ds_read_b64_tr_b16 v[58:59], v22 offset:2912\n" \
  "ds_read_b128 v[60:63], v21\n" \
  "v_add_u32 v22, 3584, v22\n" \
  "v_add_u32 v18, 16384, v18\n" \
  "v_add_u32 v19, 16384, v19\n" \
  "v_add_u32 v20, 16384, v20\n" \
  "v_add_u32 v21, 16384, v21\n" \
  ".Lfz_sa_body:\n" \
  "s_waitcnt lgkmcnt(0)\n" \
  "s_barrier\n" \
  "s_cmp_eq_u32 s47, 0\n" \
  "s_cbranch_scc1 .Lfz_sa_ka\n" \
  "v_mfma_f32_32x32x16_bf16 v[128:143], v[36:39], v[32:35], v[128:143]\n" \
  "ds_read_b64_tr_b16 v[64:65], v22 offset:0\n" \
  "ds_read_b64_tr_b16 v[66:67], v22 offset:224\n" \
  "ds_read_b128 v[68:71], v18\n" \
  "v_mfma_f32_32x32x16_bf16 v[128:143], v[44:47], v[40:43], v[128:143]\n" \
  "ds_read_b64_tr_b16 v[72:73], v22 offset:896\n" \
  "ds_read_b64_tr_b16 v[74:75], v22 offset:1120\n" \
  "ds_read_b128 v[76:79], v19\n" \
  "v_mfma_f32_32x32x16_bf16 v[128:143], v[52:55], v[48:51], v[128:143]\n" \
  "ds_read_b64_tr_b16 v[80:81], v22 offset:1792\n" \
  "ds_read_b64_tr_b16 v[82:83], v22 offset:2016\n" \
  "ds_read_b128 v[84:87], v20\n" \
  "v_mfma_f32_32x32x16_bf16 v[128:143], v[60:63], v[56:59], v[128:143]\n" \
  "ds_read_b64_tr_b16 v[88:89], v22 offset:2688\n" \
  "ds_read_b64_tr_b16 v[90:91], v22 offset:2912\n" \
  "ds_read_b128 v[92:95], v21\n" \
  "s_branch .Lfz_sa_da\n" \
  ".Lfz_sa_ka:\n" \
  "v_mfma_f32_32x32x16_bf16 v[128:143], v[32:35], v[36:39], v[128:143]\n" \
  "ds_read_b64_tr_b16 v[64:65], v22 offset:0\n" \
  "ds_read_b64_tr_b16 v[66:67], v22 offset:224\n" \
  "ds_read_b128 v[68:71], v18\n" \
  "v_mfma_f32_32x32x16_bf16 v[128:143], v[40:43], v[44:47], v[128:143]\n" \
  "ds_read_b64_tr_b16 v[72:73], v22 offset:896\n" \
  "ds_read_b64_tr_b16 v[74:75], v22 offset:1120\n" \
  "ds_read_b128 v[76:79], v19\n" \
  "v_mfma_f32_32x32x16_bf16 v[128:143], v[48:51], v[52:55], v[128:143]\n" \
  "ds_read_b64_tr_b16 v[80:81], v22 offset:1792\n" \
  "ds_read_b64_tr_b16 v[82:83], v22 offset:2016\n" \
  "ds_read_b128 v[84:87], v20\n" \
  "v_mfma_f32_32x32x16_bf16 v[128:143], v[56:59], v[60:63], v[128:143]\n" \
  "ds_read_b64_tr_b16 v[88:89], v22 offset:2688\n" \
  "ds_read_b64_tr_b16 v[90:91], v22 offset:2912\n" \
  "ds_read_b128 v[92:95], v21\n" \
  ".Lfz_sa_da:\n" \
  "v_add_u32 v22, 3584, v22\n" \
  "v_add_u32 v18, 16384, v18\n" \
  "v_add_u32 v19, 16384, v19\n" \
  "v_add_u32 v20, 16384, v20\n" \
  "v_add_u32 v21, 16384, v21\n" \
  "v_dot2c_f32_bf16 v160, s56, v36\n" \
  "v_dot2c_f32_bf16 v164, v36, v36\n" \
  "v_dot2c_f32_bf16 v168, s56, v32\n" \
  "v_dot2c_f32_bf16 v161, s56, v37\n" \
  "v_dot2c_f32_bf16 v165, v37, v37\n" \
  "v_dot2c_f32_bf16 v169, s56, v33\n" \
  "v_dot2c_f32_bf16 v162, s56, v38\n" \
  "v_dot2c_f32_bf16 v166, v38, v38\n" \
  "v_dot2c_f32_bf16 v170, s56, v34\n" \
  "v_dot2c_f32_bf16 v163, s56, v39\n" \
  "v_dot2c_f32_bf16 v167, v39, v39\n" \
  "v_dot2c_f32_bf16 v171, s56, v35\n" \
  "v_dot2c_f32_bf16 v160, s56, v44\n" \
  "v_dot2c_f32_bf16 v164, v44, v44\n" \
  "v_dot2c_f32_bf16 v168, s56, v40\n" \
  "v_dot2c_f32_bf16 v161, s56, v45\n" \
  "v_dot2c_f32_bf16 v165, v45, v45\n" \
  "v_dot2c_f32_bf16 v169, s56, v41\n" \
  "v_dot2c_f32_bf16 v162, s56, v46\n" \
  "v_dot2c_f32_bf16 v166, v46, v46\n" \
  "v_dot2c_f32_bf16 v170, s56, v42\n" \
  "v_dot2c_f32_bf16 v163, s56, v47\n" \
  "v_dot2c_f32_bf16 v167, v47, v47\n" \
  "v_dot2c_f32_bf16 v171, s56, v43\n" \
  "v_dot2c_f32_bf16 v160, s56, v52\n" \
  "v_dot2c_f32_bf16 v164, v52, v52\n" \
  "v_dot2c_f32_bf16 v168, s56, v48\n" \
  "v_dot2c_f32_bf16 v161, s56, v53\n" \
  "v_dot2c_f32_bf16 v165, v53, v53\n" \
  "v_dot2c_f32_bf16 v169, s56, v49\n" \
  "v_dot2c_f32_bf16 v162, s56, v54\n" \
  "v_dot2c_f32_bf16 v166, v54, v54\n" \
  "v_dot2c_f32_bf16 v170, s56, v50\n" \
  "v_dot2c_f32_bf16 v163, s56, v55\n" \
  "v_dot2c_f32_bf16 v167, v55, v55\n" \
  "v_dot2c_f32_bf16 v171, s56, v51\n" \
  "v_dot2c_f32_bf16 v160, s56, v60\n" \
  "v_dot2c_f32_bf16 v164, v60, v60\n" \
  "v_dot2c_f32_bf16 v168, s56, v56\n" \
  "v_dot2c_f32_bf16 v161, s56, v61\n" \
  "v_dot2c_f32_bf16 v165, v61, v61\n" \
  "v_dot2c_f32_bf16 v169, s56, v57\n" \
  "v_dot2c_f32_bf16 v162, s56, v62\n" \
  "v_dot2c_f32_bf16 v166, v62, v62\n" \
  "v_dot2c_f32_bf16 v170, s56, v58\n" \
  "v_dot2c_f32_bf16 v163, s56, v63\n" \
  "v_dot2c_f32_bf16 v167, v63, v63\n" \
  "v_dot2c_f32_bf16 v171, s56, v59\n" \
  "s_waitcnt lgkmcnt(0)\n" \
  "s_barrier\n" \
  "s_cmp_eq_u32 s47, 0\n" \
  "s_cbranch_scc1 .Lfz_sa_kb\n" \
  "v_mfma_f32_32x32x16_bf16 v[128:143], v[68:71], v[64:67], v[128:143]\n" \
  "ds_read_b64_tr_b16 v[32:33], v22 offset:0\n" \
  "ds_read_b64_tr_b16 v[34:35], v22 offset:224\n" \
  "ds_read_b128 v[36:39], v18\n" \
  "v_mfma_f32_32x32x16_bf16 v[128:143], v[76:79], v[72:75], v[128:143]\n" \
  "ds_read_b64_tr_b16 v[40:41], v22 offset:896\n" \
  "ds_read_b64_tr_b16 v[42:43], v22 offset:1120\n" \
  "ds_read_b128 v[44:47], v19\n" \
  "v_mfma_f32_32x32x16_bf16 v[128:143], v[84:87], v[80:83], v[128:143]\n" \
  "ds_read_b64_tr_b16 v[48:49], v22 offset:1792\n" \
  "ds_read_b64_tr_b16 v[50:51], v22 offset:2016\n" \
  "ds_read_b128 v[52:55], v20\n" \
  "v_mfma_f32_32x32x16_bf16 v[128:143], v[92:95], v[88:91], v[128:143]\n" \
  "ds_read_b64_tr_b16 v[56:57], v22 offset:2688\n" \
  "ds_read_b64_tr_b16 v[58:59], v22 offset:2912\n" \
  "ds_read_b128 v[60:63], v21\n" \
  "s_branch .Lfz_sa_db\n" \
  ".Lfz_sa_kb:\n" \
  "v_mfma_f32_32x32x16_bf16 v[128:143], v[64:67], v[68:71], v[128:143]\n" \
  "ds_read_b64_tr_b16 v[32:33], v22 offset:0\n" \
  "ds_read_b64_tr_b16 v[34:35], v22 offset:224\n" \
  "ds_read_b128 v[36:39], v18\n" \
  "v_mfma_f32_32x32x16_bf16 v[128:143], v[72:75], v[76:79], v[128:143]\n" \
  "ds_read_b64_tr_b16 v[40:41], v22 offset:896\n" \
  "ds_read_b64_tr_b16 v[42:43], v22 offset:1120\n" \
  "ds_read_b128 v[44:47], v19\n" \
  "v_mfma_f32_32x32x16_bf16 v[128:143], v[80:83], v[84:87], v[128:143]\n" \
  "ds_read_b64_tr_b16 v[48:49], v22 offset:1792\n" \
  "ds_read_b64_tr_b16 v[50:51], v22 offset:2016\n" \
  "ds_read_b128 v[52:55], v20\n" \
  "v_mfma_f32_32x32x16_bf16 v[128:143], v[88:91], v[92:95], v[128:143]\n" \
  "ds_read_b64_tr_b16 v[56:57], v22 offset:2688\n" \
  "ds_read_b64_tr_b16 v[58:59], v22 offset:2912\n" \
  "ds_read_b128 v[60:63], v21\n" \
  ".Lfz_sa_db:\n" \
  "v_add_u32 v22, 3584, v22\n" \
  "v_add_u32 v18, 16384, v18\n" \
  "v_add_u32 v19, 16384, v19\n" \
  "v_add_u32 v20, 16384, v20\n" \
  "v_add_u32 v21, 16384, v21\n" \
  "v_dot2c_f32_bf16 v160, s56, v68\n" \
  "v_dot2c_f32_bf16 v164, v68, v68\n" \
  "v_dot2c_f32_bf16 v168, s56, v64\n" \
  "v_dot2c_f32_bf16 v161, s56, v69\n" \
  "v_dot2c_f32_bf16 v165, v69, v69\n" \
  "v_dot2c_f32_bf16 v169, s56, v65\n" \
  "v_dot2c_f32_bf16 v162, s56, v70\n" \
  "v_dot2c_f32_bf16 v166, v70, v70\n" \
  "v_dot2c_f32_bf16 v170, s56, v66\n" \
  "v_dot2c_f32_bf16 v163, s56, v71\n" \
  "v_dot2c_f32_bf16 v167, v71, v71\n" \
  "v_dot2c_f32_bf16 v171, s56, v67\n" \
  "v_dot2c_f32_bf16 v160, s56, v76\n" \
  "v_dot2c_f32_bf16 v164, v76, v76\n" \
  "v_dot2c_f32_bf16 v168, s56, v72\n" \
  "v_dot2c_f32_bf16 v161, s56, v77\n" \
  "v_dot2c_f32_bf16 v165, v77, v77\n" \
  "v_dot2c_f32_bf16 v169, s56, v73\n" \
  "v_dot2c_f32_bf16 v162, s56, v78\n" \
  "v_dot2c_f32_bf16 v166, v78, v78\n" \
  "v_dot2c_f32_bf16 v170, s56, v74\n" \
  "v_dot2c_f32_bf16 v163, s56, v79\n" \
  "v_dot2c_f32_bf16 v167, v79, v79\n" \
  "v_dot2c_f32_bf16 v171, s56, v75\n" \
  "v_dot2c_f32_bf16 v160, s56, v84\n" \
  "v_dot2c_f32_bf16 v164, v84, v84\n" \
  "v_dot2c_f32_bf16 v168, s56, v80\n" \
  "v_dot2c_f32_bf16 v161, s56, v85\n" \
  "v_dot2c_f32_bf16 v165, v85, v85\n" \
  "v_dot2c_f32_bf16 v169, s56, v81\n" \
  "v_dot2c_f32_bf16 v162, s56, v86\n" \
  "v_dot2c_f32_bf16 v166, v86, v86\n" \
  "v_dot2c_f32_bf16 v170, s56, v82\n" \
  "v_dot2c_f32_bf16 v163, s56, v87\n" \
  "v_dot2c_f32_bf16 v167, v87, v87\n" \
  "v_dot2c_f32_bf16 v171, s56, v83\n" \
  "v_dot2c_f32_bf16 v160, s56, v92\n" \
  "v_dot2c_f32_bf16 v164, v92, v92\n" \
  "v_dot2c_f32_bf16 v168, s56, v88\n" \
  "v_dot2c_f32_bf16 v161, s56, v93\n" \
  "v_dot2c_f32_bf16 v165, v93, v93\n" \
  "v_dot2c_f32_bf16 v169, s56, v89\n" \
  "v_dot2c_f32_bf16 v162, s56, v94\n" \
  "v_dot2c_f32_bf16 v166, v94, v94\n" \
  "v_dot2c_f32_bf16 v170, s56, v90\n" \
  "v_dot2c_f32_bf16 v163, s56, v95\n" \
  "v_dot2c_f32_bf16 v167, v95, v95\n" \
  "v_dot2c_f32_bf16 v171, s56, v91\n" \
  "s_sub_u32 s54, s54, 1\n" \
  "s_cmp_lg_u32 s54, 0\n" \
  "s_cbranch_scc1 .Lfz_sa_body\n" \
  "s_waitcnt lgkmcnt(0)\n" \
  "s_barrier\n" \
  "s_cmp_eq_u32 s47, 0\n" \
  "s_cbranch_scc1 .Lfz_sa_kz\n" \
  "v_mfma_f32_32x32x16_bf16 v[128:143], v[36:39], v[32:35], v[128:143]\n" \
  "v_mfma_f32_32x32x16_bf16 v[128:143], v[44:47], v[40:43], v[128:143]\n" \
  "v_mfma_f32_32x32x16_bf16 v[128:143], v[52:55], v[48:51], v[128:143]\n" \
  "v_mfma_f32_32x32x16_bf16 v[128:143], v[60:63], v[56:59], v[128:143]\n" \
  "s_branch .Lfz_sa_dz\n" \
  ".Lfz_sa_kz:\n" \
  "v_mfma_f32_32x32x16_bf16 v[128:143], v[32:35], v[36:39], v[128:143]\n" \
  "v_mfma_f32_32x32x16_bf16 v[128:143], v[40:43], v[44:47], v[128:143]\n" \
  "v_mfma_f32_32x32x16_bf16 v[128:143], v[48:51], v[52:55], v[128:143]\n" \
  "v_mfma_f32_32x32x16_bf16 v[128:143], v[56:59], v[60:63], v[128:143]\n" \
  ".Lfz_sa_dz:\n" \
  "v_dot2c_f32_bf16 v160, s56, v36\n" \
  "v_dot2c_f32_bf16 v164, v36, v36\n" \
  "v_dot2c_f32_bf16 v168, s56, v32\n" \
  "v_dot2c_f32_bf16 v161, s56, v37\n" \
  "v_dot2c_f32_bf16 v165, v37, v37\n" \
  "v_dot2c_f32_bf16 v169, s56, v33\n" \
  "v_dot2c_f32_bf16 v162, s56, v38\n" \
  "v_dot2c_f32_bf16 v166, v38, v38\n" \
  "v_dot2c_f32_bf16 v170, s56, v34\n" \
  "v_dot2c_f32_bf16 v163, s56, v39\n" \
  "v_dot2c_f32_bf16 v167, v39, v39\n" \
  "v_dot2c_f32_bf16 v171, s56, v35\n" \
  "v_dot2c_f32_bf16 v160, s56, v44\n" \
  "v_dot2c_f32_bf16 v164, v44, v44\n" \
  "v_dot2c_f32_bf16 v168, s56, v40\n" \
  "v_dot2c_f32_bf16 v161, s56, v45\n" \
  "v_dot2c_f32_bf16 v165, v45, v45\n" \
  "v_dot2c_f32_bf16 v169, s56, v41\n" \
  "v_dot2c_f32_bf16 v162, s56, v46\n" \
  "v_dot2c_f32_bf16 v166, v46, v46\n" \
  "v_dot2c_f32_bf16 v170, s56, v42\n" \
  "v_dot2c_f32_bf16 v163, s56, v47\n" \
  "v_dot2c_f32_bf16 v167, v47, v47\n" \
  "v_dot2c_f32_bf16 v171, s56, v43\n" \
  "v_dot2c_f32_bf16 v160, s56, v52\n" \
  "v_dot2c_f32_bf16 v164, v52, v52\n" \
  "v_dot2c_f32_bf16 v168, s56, v48\n" \
  "v_dot2c_f32_bf16 v161, s56, v53\n" \
  "v_dot2c_f32_bf16 v165, v53, v53\n" \
  "v_dot2c_f32_bf16 v169, s56, v49\n" \
  "v_dot2c_f32_bf16 v162, s56, v54\n" \
  "v_dot2c_f32_bf16 v166, v54, v54\n" \
  "v_dot2c_f32_bf16 v170, s56, v50\n" \
  "v_dot2c_f32_bf16 v163, s56, v55\n" \
  "v_dot2c_f32_bf16 v167, v55, v55\n" \
  "v_dot2c_f32_bf16 v171, s56, v51\n" \
  "v_dot2c_f32_bf16 v160, s56, v60\n" \
  "v_dot2c_f32_bf16 v164, v60, v60\n" \
  "v_dot2c_f32_bf16 v168, s56, v56\n" \
  "v_dot2c_f32_bf16 v161, s56, v61\n" \
  "v_dot2c_f32_bf16 v165, v61, v61\n" \
  "v_dot2c_f32_bf16 v169, s56, v57\n" \
  "v_dot2c_f32_bf16 v162, s56, v62\n" \
  "v_dot2c_f32_bf16 v166, v62, v62\n" \
  "v_dot2c_f32_bf16 v170, s56, v58\n" \
  "v_dot2c_f32_bf16 v163, s56, v63\n" \
  "v_dot2c_f32_bf16 v167, v63, v63\n" \
  "v_dot2c_f32_bf16 v171, s56, v59\n" \
  "v_add_f32 v96, v160, v161\n" \
  "v_add_f32 v97, v162, v163\n" \
  "v_add_f32 v98, v164, v165\n" \
  "v_add_f32 v99, v166, v167\n" \
  "v_add_f32 v100, v168, v169\n" \
  "v_add_f32 v101, v170, v171\n" \
  "v_add_f32 v96, v96, v97\n" \
  "v_add_f32 v98, v98, v99\n" \
  "v_add_f32 v100, v100, v101\n" \
  "v_mov_b32 v97, v96\n" \
  "s_nop 1\n" \
  "v_permlane32_swap_b32 v96, v97\n" \
  "v_add_f32 v96, v96, v97\n" \
  "v_mov_b32 v99, v98\n" \
  "s_nop 1\n" \
  "v_permlane32_swap_b32 v98, v99\n" \
  "v_add_f32 v98, v98, v99\n" \
  "v_mov_b32 v101, v100\n" \
  "s_nop 1\n" \
  "v_permlane32_swap_b32 v100, v101\n" \
  "v_add_f32 v100, v100, v101\n" \
    \
  "v_mul_f32 v96, 0x3b124925, v96\n" \
  "v_mul_f32 v98, 0x3b124925, v98\n" \
  "v_fma_f32 v98, -v96, v96, v98\n" \
  "v_max_f32 v98, 0, v98\n" \
  "v_cvt_pk_bf16_f32 v96, -v96, v98\n" \
  "v_and_b32 v98, 0xffff0000, v96\n" \
  "v_add_f32 v98, 0x3727c5ac, v98\n" \
  "v_cvt_pk_bf16_f32 v98, v98, v98\n" \
  "v_and_b32 v98, 0xffff0000, v98\n" \
  "v_sqrt_f32 v98, v98\n" \
  "s_mov_b32 s53, 0xffff\n" \
  "v_cvt_pk_bf16_f32 v98, v98, v98\n" \
  "v_bfi_b32 v242, s53, v96, v98\n" \
  "v_mov_b32 v243, s51\n" \
  "v_cmp_eq_u32 vcc, 0, v4\n" \
  "v_and_b32 v39, 0xffff0000, v242\n" \
  "v_and_b32 v40, 0xffff, v242\n" \
  "v_rcp_f32 v39, v39\n" \
  "v_lshl_or_b32 v40, v40, 16, v40\n" \
  "v_cndmask_b32 v40, 0, v40, vcc\n" \
  "v_mov_b32 v41, v40\n" \
  "v_mov_b64 v[42:43], 0\n" \
  "v_and_b32 v34, 0xffff0000, v100\n" \
  "v_sub_f32 v100, v100, v34\n" \
  "v_and_b32 v35, 0xffff0000, v100\n" \
  "v_sub_f32 v100, v100, v35\n" \
  "v_perm_b32 v32, v35, v34, s62\n" \
  "v_lshrrev_b32 v33, 16, v100\n" \
  "v_mov_b64 v[34:35], 0\n" \
  "s_mov_b32 s32, 0x0fffffff\n" \
  "s_mov_b32 s33, 0x0fffffff\n" \
  "s_lshl2_add_u32 s35, s46, s45\n" \
  "s_lshl_b32 s35, s35, 8\n" \
  "s_add_u32 s35, s35, 9699328\n" \
  "s_lshl2_add_u32 s34, s46, s45\n" \
  "s_mul_i32 s34, s34, 144\n" \
  "s_lshl1_add_u32 s34, s49, s34\n" \
  "s_add_u32 s34, s34, 112\n" \
  "s_lshl_b32 s34, s34, 10\n" \
  "s_lshl2_add_u32 s53, s46, s45\n" \
  "s_lshl_b32 s53, s53, 12\n" \
  "s_add_u32 s53, s53, 9437184\n" \
  "s_cmp_eq_u32 s47, 0\n" \
  "s_cbranch_scc0 .Lfz_sa_v\n" \
  "v_mfma_f32_32x32x16_bf16 v[128:143], v[32:35], v[40:43], v[128:143]\n" \
  "v_lshlrev_b32 v22, 3, v3\n" \
  "buffer_store_dwordx2 v[242:243], v22, s[36:39], s35 offen sc1\n" \
  "v_and_b32 v22, 0x13, v0\n" \
  "v_bfe_u32 v23, v0, 2, 1\n" \
  "v_lshl_add_u32 v22, v23, 3, v22\n" \
  "v_bfe_u32 v23, v0, 3, 1\n" \
  "v_lshl_add_u32 v22, v23, 2, v22\n" \
  "v_lshlrev_b32 v23, 3, v22\n" \
  "v_lshlrev_b32 v22, 4, v22\n" \
  "v_lshl_add_u32 v24, v4, 9, v22\n" \
  "s_nop 1\n" \
  "v_mul_f32 v128, v39, v128\n" \
  "v_mul_f32 v129, v39, v129\n" \
  "v_mul_f32 v130, v39, v130\n" \
  "v_mul_f32 v131, v39, v131\n" \
  "v_mul_f32 v132, v39, v132\n" \
  "v_mul_f32 v133, v39, v133\n" \
  "v_mul_f32 v134, v39, v134\n" \
  "v_mul_f32 v135, v39, v135\n" \
  "v_mul_f32 v136, v39, v136\n" \
  "v_mul_f32 v137, v39, v137\n" \
  "v_mul_f32 v138, v39, v138\n" \
  "v_mul_f32 v139, v39, v139\n" \
  "v_mul_f32 v140, v39, v140\n" \
  "v_mul_f32 v141, v39, v141\n" \
  "v_mul_f32 v142, v39, v142\n" \
  "v_mul_f32 v143, v39, v143\n" \
  "v_cvt_pk_bf16_f32 v48, v128, v129\n" \
  "v_cvt_pk_bf16_f32 v49, v130, v131\n" \
  "v_cvt_pk_bf16_f32 v50, v132, v133\n" \
  "v_cvt_pk_bf16_f32 v51, v134, v135\n" \
  "v_cvt_pk_bf16_f32 v52, v136, v137\n" \
  "v_cvt_pk_bf16_f32 v53, v138, v139\n" \
  "v_cvt_pk_bf16_f32 v54, v140, v141\n" \
  "v_cvt_pk_bf16_f32 v55, v142, v143\n" \
  "buffer_store_dwordx4 v[48:51], v24, s[36:39], s53 offen sc1\n" \
  "s_mov_b64 exec, vcc\n" \
  "buffer_store_dwordx4 v[52:55], v22, s[36:39], s53 offen offset:1280 sc1\n" \
  "s_not_b64 exec, vcc\n" \
  "buffer_store_dwordx2 v[52:53], v23, s[36:39], s53 offen offset:1024 sc1\n" \
  "s_branch .Lfz_sa_st\n" \
  ".Lfz_sa_v:\n" \
  "v_mfma_f32_32x32x16_bf16 v[128:143], v[40:43], v[32:35], v[128:143]\n" \
  "s_lshl_b32 s54, s45, 8\n" \
  "s_add_u32 s54, s54, 155648\n" \
  "v_lshl_add_u32 v22, v1, 2, s54\n" \
  "v_lshl_add_u32 v23, v4, 4, s54\n" \
  "ds_write_b32 v22, v39\n" \
  "ds_read_b128 v[160:163], v23 offset:0\n" \
  "ds_read_b128 v[164:167], v23 offset:32\n" \
  "ds_read_b128 v[168:171], v23 offset:64\n" \
  "ds_read_b128 v[172:175], v23 offset:96\n" \
  "v_cmp_gt_u32 vcc, 28, v3\n" \
  "v_lshlrev_b32 v22, 6, v4\n" \
  "v_sub_u32 v22, v2, v22\n" \
  "s_waitcnt lgkmcnt(0)\n" \
  "v_mul_f32 v128, v160, v128\n" \
  "v_mul_f32 v129, v161, v129\n" \
  "v_mul_f32 v130, v162, v130\n" \
  "v_mul_f32 v131, v163, v131\n" \
  "v_mul_f32 v132, v164, v132\n" \
  "v_mul_f32 v133, v165, v133\n" \
  "v_mul_f32 v134, v166, v134\n" \
  "v_mul_f32 v135, v167, v135\n" \
  "v_mul_f32 v136, v168, v136\n" \
  "v_mul_f32 v137, v169, v137\n" \
  "v_mul_f32 v138, v170, v138\n" \
  "v_mul_f32 v139, v171, v139\n" \
  "v_mul_f32 v140, v172, v140\n" \
  "v_mul_f32 v141, v173, v141\n" \
  "v_mul_f32 v142, v174, v142\n" \
  "v_mul_f32 v143, v175, v143\n" \
  "v_cvt_pk_bf16_f32 v48, v128, v129\n" \
  "v_cvt_pk_bf16_f32 v49, v130, v131\n" \
  "v_cvt_pk_bf16_f32 v50, v132, v133\n" \
  "v_cvt_pk_bf16_f32 v51, v134, v135\n" \
  "v_cvt_pk_bf16_f32 v52, v136, v137\n" \
  "v_cvt_pk_bf16_f32 v53, v138, v139\n" \
  "v_cvt_pk_bf16_f32 v54, v140, v141\n" \
  "v_cvt_pk_bf16_f32 v55, v142, v143\n" \
  "s_mov_b64 exec, vcc\n" \
  "buffer_store_dwordx4 v[48:51], v22, s[36:39], s53 offen offset:1792 sc1\n" \
  "buffer_store_dwordx4 v[52:55], v22, s[36:39], s53 offen offset:2688 sc1\n" \
  ".Lfz_sa_st:\n" \
  "s_mov_b64 exec, -1\n" \
  "v_mov_b64 v[44:45], -1\n" \
  "v_mov_b64 v[46:47], -1\n" \
  "buffer_store_dwordx4 v[44:47], v2, s[36:39], s34 offen sc1\n" \
  "buffer_store_dwordx4 v[44:47], v2, s[36:39], s34 offen offset:1024 sc1\n" \
  "s_barrier\n" \
  "s_lshl1_add_u32 s54, s46, s47\n" \
  "s_lshl_b32 s54, s54, 7\n" \
  "s_add_u32 s54, s54, 9945088\n" \
  "v_add_u32 v18, 4294852608, v18\n" \
  "v_add_u32 v19, 4294852608, v19\n" \
  "v_add_u32 v20, 4294852608, v20\n" \
  "v_add_u32 v21, 4294852608, v21\n" \
  "v_mov_b64 v[128:129], 0\n" \
  "v_mov_b64 v[130:131], 0\n" \
  "v_mov_b64 v[132:133], 0\n" \
  "v_mov_b64 v[134:135], 0\n" \
  "v_mov_b64 v[136:137], 0\n" \
  "v_mov_b64 v[138:139], 0\n" \
  "v_mov_b64 v[140:141], 0\n" \
  "v_mov_b64 v[142:143], 0\n" \
  "v_mov_b64 v[96:97], 0\n" \
  "v_mov_b64 v[98:99], 0\n" \
  "v_mov_b64 v[100:101], 0\n" \
  "v_mov_b64 v[102:103], 0\n" \
  "v_mov_b64 v[104:105], 0\n" \
  "v_mov_b64 v[106:107], 0\n" \
  "v_mov_b64 v[108:109], 0\n" \
  "v_mov_b64 v[110:111], 0\n" \
  "v_mov_b64 v[112:113], 0\n" \
  "v_mov_b64 v[114:115], 0\n" \
  "v_mov_b64 v[116:117], 0\n" \
  "v_mov_b64 v[118:119], 0\n" \
  "v_mov_b64 v[120:121], 0\n" \
  "v_mov_b64 v[122:123], 0\n" \
  "v_mov_b64 v[124:125], 0\n" \
  "v_mov_b64 v[126:127], 0\n" \
  "s_waitcnt vmcnt(0)\n" \
  "s_mov_b64 exec, 1\n" \
  "v_mov_b32 v23, 159748\n" \
  "v_mov_b32 v24, 1\n" \
  "ds_add_rtn_u32 v24, v23, v24\n" \
  "v_mov_b32 v23, 0\n" \
  "s_waitcnt lgkmcnt(0)\n" \
  "v_readfirstlane_b32 s63, v24\n" \
  "v_mov_b32 v24, s51\n" \
  "s_cmp_lg_u32 s63, 3\n" \
  "s_cbranch_scc1 .Lfz_sig2\n" \
  "buffer_store_dword v24, v23, s[36:39], s54 offen sc1\n" \
  ".Lfz_sig2:\n" \
  "s_mov_b64 exec, -1\n" \
  "s_barrier\n" \
  "ds_read_b64_tr_b16 v[32:33], v17 offset:0\n" \
  "ds_read_b64_tr_b16 v[34:35], v17 offset:256\n" \
  "ds_read_b128 v[36:39], v18\n" \
  "ds_read_b64_tr_b16 v[40:41], v17 offset:1024\n" \
  "ds_read_b64_tr_b16 v[42:43], v17 offset:1280\n" \
  "ds_read_b128 v[44:47], v19\n" \
  "ds_read_b64_tr_b16 v[48:49], v17 offset:2048\n" \
  "ds_read_b64_tr_b16 v[50:51], v17 offset:2304\n" \
  "ds_read_b128 v[52:55], v20\n" \
  "ds_read_b64_tr_b16 v[56:57], v17 offset:3072\n" \
  "ds_read_b64_tr_b16 v[58:59], v17 offset:3328\n" \
  "ds_read_b128 v[60:63], v21\n" \
  "v_add_u32 v17, 4096, v17\n" \
  "v_add_u32 v18, 16384, v18\n" \
  "v_add_u32 v19, 16384, v19\n" \
  "v_add_u32 v20, 16384, v20\n" \
  "v_add_u32 v21, 16384, v21\n" \
  "s_mov_b32 s54, 3\n" \
  ".Lfz_sa_q:\n" \
  "s_waitcnt lgkmcnt(0)\n" \
  "v_mfma_f32_32x32x16_bf16 v[144:159], v[32:35], v[36:39], v[144:159]\n" \
  "ds_read_b64_tr_b16 v[64:65], v17 offset:0\n" \
  "ds_read_b64_tr_b16 v[66:67], v17 offset:256\n" \
  "ds_read_b128 v[68:71], v18\n" \
  "v_mfma_f32_32x32x16_bf16 v[144:159], v[40:43], v[44:47], v[144:159]\n" \
  "ds_read_b64_tr_b16 v[72:73], v17 offset:1024\n" \
  "ds_read_b64_tr_b16 v[74:75], v17 offset:1280\n" \
  "ds_read_b128 v[76:79], v19\n" \
  "v_mfma_f32_32x32x16_bf16 v[144:159], v[48:51], v[52:55], v[144:159]\n" \
  "ds_read_b64_tr_b16 v[80:81], v17 offset:2048\n" \
  "ds_read_b64_tr_b16 v[82:83], v17 offset:2304\n" \
  "ds_read_b128 v[84:87], v20\n" \
  "v_mfma_f32_32x32x16_bf16 v[144:159], v[56:59], v[60:63], v[144:159]\n" \
  "ds_read_b64_tr_b16 v[88:89], v17 offset:3072\n" \
  "ds_read_b64_tr_b16 v[90:91], v17 offset:3328\n" \
  "ds_read_b128 v[92:95], v21\n" \
  "v_mfma_f32_32x32x16_bf16 v[128:143], v[196:199], v[32:35], v[128:143]\n" \
  "v_add_u32 v17, 4096, v17\n" \
  "v_add_u32 v18, 16384, v18\n" \
  "v_add_u32 v19, 16384, v19\n" \
  "v_add_u32 v20, 16384, v20\n" \
  "v_add_u32 v21, 16384, v21\n" \
  "v_mfma_f32_32x32x16_bf16 v[128:143], v[196:199], v[40:43], v[128:143]\n" \
  "v_mfma_f32_32x32x16_bf16 v[128:143], v[196:199], v[48:51], v[128:143]\n" \
  "v_mfma_f32_32x32x16_bf16 v[128:143], v[196:199], v[56:59], v[128:143]\n" \
  "s_waitcnt lgkmcnt(0)\n" \
  "v_mfma_f32_32x32x16_bf16 v[144:159], v[64:67], v[68:71], v[144:159]\n" \
  "ds_read_b64_tr_b16 v[32:33], v17 offset:0\n" \
  "ds_read_b64_tr_b16 v[34:35], v17 offset:256\n" \
  "ds_read_b128 v[36:39], v18\n" \
  "v_mfma_f32_32x32x16_bf16 v[144:159], v[72:75], v[76:79], v[144:159]\n" \
  "ds_read_b64_tr_b16 v[40:41], v17 offset:1024\n" \
  "ds_read_b64_tr_b16 v[42:43], v17 offset:1280\n" \
  "ds_read_b128 v[44:47], v19\n" \
  "v_mfma_f32_32x32x16_bf16 v[144:159], v[80:83], v[84:87], v[144:159]\n" \
  "ds_read_b64_tr_b16 v[48:49], v17 offset:2048\n" \
  "ds_read_b64_tr_b16 v[50:51], v17 offset:2304\n" \
  "ds_read_b128 v[52:55], v20\n" \
  "v_mfma_f32_32x32x16_bf16 v[144:159], v[88:91], v[92:95], v[144:159]\n" \
  "ds_read_b64_tr_b16 v[56:57], v17 offset:3072\n" \
  "ds_read_b64_tr_b16 v[58:59], v17 offset:3328\n" \
  "ds_read_b128 v[60:63], v21\n" \
  "v_mfma_f32_32x32x16_bf16 v[128:143], v[196:199], v[64:67], v[128:143]\n" \
  "v_add_u32 v17, 4096, v17\n" \
  "v_add_u32 v18, 16384, v18\n" \
  "v_add_u32 v19, 16384, v19\n" \
  "v_add_u32 v20, 16384, v20\n" \
  "v_add_u32 v21, 16384, v21\n" \
  "v_mfma_f32_32x32x16_bf16 v[128:143], v[196:199], v[72:75], v[128:143]\n" \
  "v_mfma_f32_32x32x16_bf16 v[128:143], v[196:199], v[80:83], v[128:143]\n" \
  "v_mfma_f32_32x32x16_bf16 v[128:143], v[196:199], v[88:91], v[128:143]\n" \
  "s_sub_u32 s54, s54, 1\n" \
  "s_cmp_lg_u32 s54, 0\n" \
  "s_cbranch_scc1 .Lfz_sa_q\n" \
  "s_waitcnt lgkmcnt(0)\n" \
  "v_mfma_f32_32x32x16_bf16 v[144:159], v[32:35], v[36:39], v[144:159]\n" \
  "v_mfma_f32_32x32x16_bf16 v[144:159], v[40:43], v[44:47], v[144:159]\n" \
  "v_mfma_f32_32x32x16_bf16 v[144:159], v[48:51], v[52:55], v[144:159]\n" \
  "v_mfma_f32_32x32x16_bf16 v[144:159], v[56:59], v[60:63], v[144:159]\n" \
  "v_mfma_f32_32x32x16_bf16 v[128:143], v[196:199], v[32:35], v[128:143]\n" \
  "v_mfma_f32_32x32x16_bf16 v[128:143], v[196:199], v[40:43], v[128:143]\n" \
  "v_mfma_f32_32x32x16_bf16 v[128:143], v[196:199], v[48:51], v[128:143]\n" \
  "v_mfma_f32_32x32x16_bf16 v[128:143], v[196:199], v[56:59], v[128:143]\n" \
  "v_mov_b64 v[64:65], 0\n" \
  "v_mov_b64 v[66:67], 0\n" \
  "v_mov_b64 v[68:69], 0\n" \
  "v_mov_b64 v[70:71], 0\n" \
  "v_mov_b64 v[72:73], 0\n" \
  "v_mov_b64 v[74:75], 0\n" \
  "v_mov_b64 v[76:77], 0\n" \
  "v_mov_b64 v[78:79], 0\n" \
  "v_mov_b64 v[80:81], 0\n" \
  "v_mov_b64 v[82:83], 0\n" \
  "v_mov_b64 v[84:85], 0\n" \
  "v_mov_b64 v[86:87], 0\n" \
  "v_mov_b64 v[88:89], 0\n" \
  "v_mov_b64 v[90:91], 0\n" \
  "v_mov_b64 v[92:93], 0\n" \
  "v_mov_b64 v[94:95], 0\n" \
  "v_and_b32 v170, 0xffff0000, v128\n" \
  "v_sub_f32 v128, v128, v170\n" \
  "v_and_b32 v171, 0xffff0000, v128\n" \
  "v_sub_f32 v128, v128, v171\n" \
  "v_perm_b32 v168, v171, v170, s62\n" \
  "v_lshrrev_b32 v169, 16, v128\n" \
  "v_mov_b64 v[170:171], 0\n" \
  "s_mov_b32 s63, 0x100000\n" \
  ".Lfz_poll3:\n" \
  "buffer_load_dword v23, v11, s[36:39], 0 offen sc1\n" \
  "s_waitcnt vmcnt(0)\n" \
  "v_cmp_ne_u32 vcc, s51, v23\n" \
  "s_sub_u32 s63, s63, 1\n" \
  "s_cbranch_vccz .Lfz_poll3x\n" \
  "s_sleep 8\n" \
  "s_cmp_lg_u32 s63, 0\n" \
  "s_cbranch_scc1 .Lfz_poll3\n" \
  ".Lfz_poll3x:\n" \
  "s_mov_b64 exec, s[32:33]\n" \
  "buffer_load_dwordx4 v[200:203], v16, s[36:39], s61 offen offset:1792 sc1\n" \
  "buffer_load_dwordx4 v[204:207], v16, s[36:39], s61 offen offset:2688 sc1\n" \
  "s_mov_b64 exec, -1\n" \
  "buffer_load_dwordx4 v[192:195], v31, s[36:39], s61 offen sc1\n" \
  "buffer_load_dwordx4 v[196:199], v10, s[36:39], s61 offen offset:1280 sc1\n" \
  "s_add_u32 s61, s61, 4096\n" \
  "s_and_b32 s61, s61, 0xffff\n" \
  "s_barrier\n" \
  "v_cmp_eq_u32 vcc, 0, v4\n" \
  "v_and_b32 v25, 0xffff0000, v242\n" \
  "v_and_b32 v160, 0xffff, v242\n" \
  "v_rcp_f32 v25, v25\n" \
  "v_lshl_or_b32 v160, v160, 16, v160\n" \
  "v_cndmask_b32 v160, 0, v160, vcc\n" \
  "v_mov_b32 v161, v160\n" \
  "v_mov_b64 v[162:163], 0\n" \
  "v_mul_f32 v24, 0x3e8b97f6, v25\n" \
  "s_nop 0\n" \
  "v_mfma_f32_32x32x16_bf16 v[144:159], v[168:171], v[160:163], v[144:159]\n" \
  "s_branch .Lfz_A_q\n" \
  ".Lfz_slow:\n" \
    \
  "s_waitcnt vmcnt(0)\n" \
  "v_mov_b32 v18, 0xff800000\n" \
  "v_mov_b32 v19, 0xff800000\n" \
  "v_mov_b32 v20, 0xff800000\n" \
  "v_mov_b32 v21, 0xff800000\n" \
  "s_mov_b32 s61, 0\n" \
  "s_mov_b32 s55, 16\n" \
  ".Lfz_sl_a:\n" \
  "buffer_load_dwordx4 v[192:195], v31, s[36:39], s61 offen sc1\n" \
  "buffer_load_dwordx4 v[196:199], v10, s[36:39], s61 offen offset:1280 sc1\n" \
  "s_add_u32 s61, s61, 4096\n" \
  "s_waitcnt vmcnt(0)\n" \
  "v_mfma_f32_32x32x16_bf16 v[128:143], v[192:195], v[32:35], 0\n" \
  "v_mfma_f32_32x32x16_bf16 v[128:143], v[196:199], v[36:39], v[128:143]\n" \
  "v_mfma_f32_32x32x16_bf16 v[144:159], v[192:195], v[40:43], 0\n" \
  "v_mfma_f32_32x32x16_bf16 v[144:159], v[196:199], v[44:47], v[144:159]\n" \
  "v_mfma_f32_32x32x16_bf16 v[160:175], v[192:195], v[48:51], 0\n" \
  "v_mfma_f32_32x32x16_bf16 v[160:175], v[196:199], v[52:55], v[160:175]\n" \
  "v_mfma_f32_32x32x16_bf16 v[176:191], v[192:195], v[56:59], 0\n" \
  "v_mfma_f32_32x32x16_bf16 v[176:191], v[196:199], v[60:63], v[176:191]\n" \
  "s_nop 5\n" \
  "v_max3_f32 v18, v128, v129, v18\n" \
  "v_max3_f32 v18, v130, v131, v18\n" \
  "v_max3_f32 v18, v132, v133, v18\n" \
  "v_max3_f32 v18, v134, v135, v18\n" \
  "v_max3_f32 v18, v136, v137, v18\n" \
  "v_max3_f32 v18, v138, v139, v18\n" \
  "v_max3_f32 v18, v140, v141, v18\n" \
  "v_max3_f32 v18, v142, v143, v18\n" \
  "v_max3_f32 v19, v144, v145, v19\n" \
  "v_max3_f32 v19, v146, v147, v19\n" \
  "v_max3_f32 v19, v148, v149, v19\n" \
  "v_max3_f32 v19, v150, v151, v19\n" \
  "v_max3_f32 v19, v152, v153, v19\n" \
  "v_max3_f32 v19, v154, v155, v19\n" \
  "v_max3_f32 v19, v156, v157, v19\n" \
  "v_max3_f32 v19, v158, v159, v19\n" \
  "v_max3_f32 v20, v160, v161, v20\n" \
  "v_max3_f32 v20, v162, v163, v20\n" \
  "v_max3_f32 v20, v164, v165, v20\n" \
  "v_max3_f32 v20, v166, v167, v20\n" \
  "v_max3_f32 v20, v168, v169, v20\n" \
  "v_max3_f32 v20, v170, v171, v20\n" \
  "v_max3_f32 v20, v172, v173, v20\n" \
  "v_max3_f32 v20, v174, v175, v20\n" \
  "v_max3_f32 v21, v176, v177, v21\n" \
  "v_max3_f32 v21, v178, v179, v21\n" \
  "v_max3_f32 v21, v180, v181, v21\n" \
  "v_max3_f32 v21, v182, v183, v21\n" \
  "v_max3_f32 v21, v184, v185, v21\n" \
  "v_max3_f32 v21, v186, v187, v21\n" \
  "v_max3_f32 v21, v188, v189, v21\n" \
  "v_max3_f32 v21, v190, v191, v21\n" \
  "s_sub_u32 s55, s55, 1\n" \
  "s_cmp_lg_u32 s55, 0\n" \
  "s_cbranch_scc1 .Lfz_sl_a\n" \
  "v_mov_b32 v26, v18\n" \
  "s_nop 1\n" \
  "v_permlane32_swap_b32 v18, v26\n" \
  "v_max_f32 v18, v18, v26\n" \
  "s_add_u32 s53, s45, 1\n" \
  "s_and_b32 s53, s53, 3\n" \
  "s_lshl_b32 s53, s53, 9\n" \
  "s_add_u32 s53, s53, 49152\n" \
  "v_lshl_add_u32 v22, v3, 2, s53\n" \
  "s_lshl_b32 s53, s45, 7\n" \
  "v_add_u32 v26, s53, v22\n" \
  "ds_write_b32 v26, v18\n" \
  "v_mov_b32 v26, v19\n" \
  "s_nop 1\n" \
  "v_permlane32_swap_b32 v19, v26\n" \
  "v_max_f32 v19, v19, v26\n" \
  "s_add_u32 s53, s45, 2\n" \
  "s_and_b32 s53, s53, 3\n" \
  "s_lshl_b32 s53, s53, 9\n" \
  "s_add_u32 s53, s53, 49152\n" \
  "v_lshl_add_u32 v23, v3, 2, s53\n" \
  "s_lshl_b32 s53, s45, 7\n" \
  "v_add_u32 v26, s53, v23\n" \
  "ds_write_b32 v26, v19\n" \
  "v_mov_b32 v26, v20\n" \
  "s_nop 1\n" \
  "v_permlane32_swap_b32 v20, v26\n" \
  "v_max_f32 v20, v20, v26\n" \
  "s_add_u32 s53, s45, 3\n" \
  "s_and_b32 s53, s53, 3\n" \
  "s_lshl_b32 s53, s53, 9\n" \
  "s_add_u32 s53, s53, 49152\n" \
  "v_lshl_add_u32 v24, v3, 2, s53\n" \
  "s_lshl_b32 s53, s45, 7\n" \
  "v_add_u32 v26, s53, v24\n" \
  "ds_write_b32 v26, v20\n" \
  "v_mov_b32 v26, v21\n" \
  "s_nop 1\n" \
  "v_permlane32_swap_b32 v21, v26\n" \
  "v_max_f32 v21, v21, v26\n" \
  "s_add_u32 s53, s45, 4\n" \
  "s_and_b32 s53, s53, 3\n" \
  "s_lshl_b32 s53, s53, 9\n" \
  "s_add_u32 s53, s53, 49152\n" \
  "v_lshl_add_u32 v25, v3, 2, s53\n" \
  "s_lshl_b32 s53, s45, 7\n" \
  "v_add_u32 v26, s53, v25\n" \
  "ds_write_b32 v26, v21\n" \
  "s_mov_b32 s53, 1\n" \
  "s_mov_b32 s60, 8\n" \
  "v_mov_b32 v9, s53\n" \
  "s_mov_b64 exec, 1\n" \
  "ds_add_u32 v8, v9\n" \
  "s_mov_b64 exec, -1\n" \
  "s_mov_b32 s63, 0x1000000\n" \
  ".Lfz_sy1:\n" \
  "ds_read_b32 v9, v8\n" \
  "s_waitcnt lgkmcnt(0)\n" \
  "v_readfirstlane_b32 s54, v9\n" \
  "s_sub_u32 s63, s63, 1\n" \
  "s_cbranch_scc1 .Lfz_sy1x\n" \
  "s_and_b32 s53, s54, 0xff\n" \
  "s_cmp_lt_u32 s53, s60\n" \
  "s_cbranch_scc1 .Lfz_sy1\n" \
  ".Lfz_sy1x:\n" \
  "ds_read2_b32 v[26:27], v22 offset0:0 offset1:32\n" \
  "ds_read2_b32 v[28:29], v22 offset0:64 offset1:96\n" \
  "s_waitcnt lgkmcnt(0)\n" \
  "v_max3_f32 v18, v26, v27, v28\n" \
  "v_max_f32 v18, v18, v29\n" \
  "ds_read2_b32 v[26:27], v23 offset0:0 offset1:32\n" \
  "ds_read2_b32 v[28:29], v23 offset0:64 offset1:96\n" \
  "s_waitcnt lgkmcnt(0)\n" \
  "v_max3_f32 v19, v26, v27, v28\n" \
  "v_max_f32 v19, v19, v29\n" \
  "ds_read2_b32 v[26:27], v24 offset0:0 offset1:32\n" \
  "ds_read2_b32 v[28:29], v24 offset0:64 offset1:96\n" \
  "s_waitcnt lgkmcnt(0)\n" \
  "v_max3_f32 v20, v26, v27, v28\n" \
  "v_max_f32 v20, v20, v29\n" \
  "ds_read2_b32 v[26:27], v25 offset0:0 offset1:32\n" \
  "ds_read2_b32 v[28:29], v25 offset0:64 offset1:96\n" \
  "s_waitcnt lgkmcnt(0)\n" \
  "v_max3_f32 v21, v26, v27, v28\n" \
  "v_max_f32 v21, v21, v29\n" \
  "v_mov_b64 v[64:65], 0\n" \
  "v_mov_b64 v[66:67], 0\n" \
  "v_mov_b64 v[68:69], 0\n" \
  "v_mov_b64 v[70:71], 0\n" \
  "v_mov_b64 v[72:73], 0\n" \
  "v_mov_b64 v[74:75], 0\n" \
  "v_mov_b64 v[76:77], 0\n" \
  "v_mov_b64 v[78:79], 0\n" \
  "v_mov_b64 v[80:81], 0\n" \
  "v_mov_b64 v[82:83], 0\n" \
  "v_mov_b64 v[84:85], 0\n" \
  "v_mov_b64 v[86:87], 0\n" \
  "v_mov_b64 v[88:89], 0\n" \
  "v_mov_b64 v[90:91], 0\n" \
  "v_mov_b64 v[92:93], 0\n" \
  "v_mov_b64 v[94:95], 0\n" \
  "v_mov_b64 v[96:97], 0\n" \
  "v_mov_b64 v[98:99], 0\n" \
  "v_mov_b64 v[100:101], 0\n" \
  "v_mov_b64 v[102:103], 0\n" \
  "v_mov_b64 v[104:105], 0\n" \
  "v_mov_b64 v[106:107], 0\n" \
  "v_mov_b64 v[108:109], 0\n" \
  "v_mov_b64 v[110:111], 0\n" \
  "v_mov_b64 v[112:113], 0\n" \
  "v_mov_b64 v[114:115], 0\n" \
  "v_mov_b64 v[116:117], 0\n" \
  "v_mov_b64 v[118:119], 0\n" \
  "v_mov_b64 v[120:121], 0\n" \
  "v_mov_b64 v[122:123], 0\n" \
  "v_mov_b64 v[124:125], 0\n" \
  "v_mov_b64 v[126:127], 0\n" \
  "s_mov_b32 s61, 0\n" \
  "s_mov_b32 s55, 16\n" \
  ".Lfz_sl_b:\n" \
  "s_mov_b64 exec, s[32:33]\n" \
  "buffer_load_dwordx4 v[200:203], v16, s[36:39], s61 offen offset:1792 sc1\n" \
  "buffer_load_dwordx4 v[204:207], v16, s[36:39], s61 offen offset:2688 sc1\n" \
  "s_mov_b64 exec, -1\n" \
  "buffer_load_dwordx4 v[192:195], v31, s[36:39], s61 offen sc1\n" \
  "buffer_load_dwordx4 v[196:199], v10, s[36:39], s61 offen offset:1280 sc1\n" \
  "s_add_u32 s61, s61, 4096\n" \
  "s_waitcnt vmcnt(0)\n" \
  "v_mfma_f32_32x32x16_bf16 v[128:143], v[192:195], v[32:35], 0\n" \
  "v_mfma_f32_32x32x16_bf16 v[128:143], v[196:199], v[36:39], v[128:143]\n" \
  "v_mfma_f32_32x32x16_bf16 v[144:159], v[192:195], v[40:43], 0\n" \
  "v_mfma_f32_32x32x16_bf16 v[144:159], v[196:199], v[44:47], v[144:159]\n" \
  "v_mfma_f32_32x32x16_bf16 v[160:175], v[192:195], v[48:51], 0\n" \
  "v_mfma_f32_32x32x16_bf16 v[160:175], v[196:199], v[52:55], v[160:175]\n" \
  "v_mfma_f32_32x32x16_bf16 v[176:191], v[192:195], v[56:59], 0\n" \
  "v_mfma_f32_32x32x16_bf16 v[176:191], v[196:199], v[60:63], v[176:191]\n" \
  "s_nop 5\n" \
  "v_sub_f32 v128, v128, v18\n" \
  "v_sub_f32 v129, v129, v18\n" \
  "v_sub_f32 v130, v130, v18\n" \
  "v_sub_f32 v131, v131, v18\n" \
  "v_sub_f32 v132, v132, v18\n" \
  "v_sub_f32 v133, v133, v18\n" \
  "v_sub_f32 v134, v134, v18\n" \
  "v_sub_f32 v135, v135, v18\n" \
  "v_sub_f32 v136, v136, v18\n" \
  "v_sub_f32 v137, v137, v18\n" \
  "v_sub_f32 v138, v138, v18\n" \
  "v_sub_f32 v139, v139, v18\n" \
  "v_sub_f32 v140, v140, v18\n" \
  "v_sub_f32 v141, v141, v18\n" \
  "v_sub_f32 v142, v142, v18\n" \
  "v_sub_f32 v143, v143, v18\n" \
  "v_exp_f32 v128, v128\n" \
  "v_exp_f32 v129, v129\n" \
  "v_exp_f32 v130, v130\n" \
  "v_exp_f32 v131, v131\n" \
  "v_exp_f32 v132, v132\n" \
  "v_exp_f32 v133, v133\n" \
  "v_exp_f32 v134, v134\n" \
  "v_exp_f32 v135, v135\n" \
  "v_exp_f32 v136, v136\n" \
  "v_exp_f32 v137, v137\n" \
  "v_exp_f32 v138, v138\n" \
  "v_exp_f32 v139, v139\n" \
  "v_exp_f32 v140, v140\n" \
  "v_exp_f32 v141, v141\n" \
  "v_exp_f32 v142, v142\n" \
  "v_exp_f32 v143, v143\n" \
  "v_smfmac_f32_32x32x32_bf16 v[64:79], v[200:203], v[128:135], v30\n" \
  "s_nop 0\n" \
  "v_smfmac_f32_32x32x32_bf16 v[64:79], v[204:207], v[136:143], v30\n" \
  "v_sub_f32 v144, v144, v19\n" \
  "v_sub_f32 v145, v145, v19\n" \
  "v_sub_f32 v146, v146, v19\n" \
  "v_sub_f32 v147, v147, v19\n" \
  "v_sub_f32 v148, v148, v19\n" \
  "v_sub_f32 v149, v149, v19\n" \
  "v_sub_f32 v150, v150, v19\n" \
  "v_sub_f32 v151, v151, v19\n" \
  "v_sub_f32 v152, v152, v19\n" \
  "v_sub_f32 v153, v153, v19\n" \
  "v_sub_f32 v154, v154, v19\n" \
  "v_sub_f32 v155, v155, v19\n" \
  "v_sub_f32 v156, v156, v19\n" \
  "v_sub_f32 v157, v157, v19\n" \
  "v_sub_f32 v158, v158, v19\n" \
  "v_sub_f32 v159, v159, v19\n" \
  "v_exp_f32 v144, v144\n" \
  "v_exp_f32 v145, v145\n" \
  "v_exp_f32 v146, v146\n" \
  "v_exp_f32 v147, v147\n" \
  "v_exp_f32 v148, v148\n" \
  "v_exp_f32 v149, v149\n" \
  "v_exp_f32 v150, v150\n" \
  "v_exp_f32 v151, v151\n" \
  "v_exp_f32 v152, v152\n" \
  "v_exp_f32 v153, v153\n" \
  "v_exp_f32 v154, v154\n" \
  "v_exp_f32 v155, v155\n" \
  "v_exp_f32 v156, v156\n" \
  "v_exp_f32 v157, v157\n" \
  "v_exp_f32 v158, v158\n" \
  "v_exp_f32 v159, v159\n" \
  "v_smfmac_f32_32x32x32_bf16 v[80:95], v[200:203], v[144:151], v30\n" \
  "s_nop 0\n" \
  "v_smfmac_f32_32x32x32_bf16 v[80:95], v[204:207], v[152:159], v30\n" \
  "v_sub_f32 v160, v160, v20\n" \
  "v_sub_f32 v161, v161, v20\n" \
  "v_sub_f32 v162, v162, v20\n" \
  "v_sub_f32 v163, v163, v20\n" \
  "v_sub_f32 v164, v164, v20\n" \
  "v_sub_f32 v165, v165, v20\n" \
  "v_sub_f32 v166, v166, v20\n" \
  "v_sub_f32 v167, v167, v20\n" \
  "v_sub_f32 v168, v168, v20\n" \
  "v_sub_f32 v169, v169, v20\n" \
  "v_sub_f32 v170, v170, v20\n" \
  "v_sub_f32 v171, v171, v20\n" \
  "v_sub_f32 v172, v172, v20\n" \
  "v_sub_f32 v173, v173, v20\n" \
  "v_sub_f32 v174, v174, v20\n" \
  "v_sub_f32 v175, v175, v20\n" \
  "v_exp_f32 v160, v160\n" \
  "v_exp_f32 v161, v161\n" \
  "v_exp_f32 v162, v162\n" \
  "v_exp_f32 v163, v163\n" \
  "v_exp_f32 v164, v164\n" \
  "v_exp_f32 v165, v165\n" \
  "v_exp_f32 v166, v166\n" \
  "v_exp_f32 v167, v167\n" \
  "v_exp_f32 v168, v168\n" \
  "v_exp_f32 v169, v169\n" \
  "v_exp_f32 v170, v170\n" \
  "v_exp_f32 v171, v171\n" \
  "v_exp_f32 v172, v172\n" \
  "v_exp_f32 v173, v173\n" \
  "v_exp_f32 v174, v174\n" \
  "v_exp_f32 v175, v175\n" \
  "v_smfmac_f32_32x32x32_bf16 v[96:111], v[200:203], v[160:167], v30\n" \
  "s_nop 0\n" \
  "v_smfmac_f32_32x32x32_bf16 v[96:111], v[204:207], v[168:175], v30\n" \
  "v_sub_f32 v176, v176, v21\n" \
  "v_sub_f32 v177, v177, v21\n" \
  "v_sub_f32 v178, v178, v21\n" \
  "v_sub_f32 v179, v179, v21\n" \
  "v_sub_f32 v180, v180, v21\n" \
  "v_sub_f32 v181, v181, v21\n" \
  "v_sub_f32 v182, v182, v21\n" \
  "v_sub_f32 v183, v183, v21\n" \
  "v_sub_f32 v184, v184, v21\n" \
  "v_sub_f32 v185, v185, v21\n" \
  "v_sub_f32 v186, v186, v21\n" \
  "v_sub_f32 v187, v187, v21\n" \
  "v_sub_f32 v188, v188, v21\n" \
  "v_sub_f32 v189, v189, v21\n" \
  "v_sub_f32 v190, v190, v21\n" \
  "v_sub_f32 v191, v191, v21\n" \
  "v_exp_f32 v176, v176\n" \
  "v_exp_f32 v177, v177\n" \
  "v_exp_f32 v178, v178\n" \
  "v_exp_f32 v179, v179\n" \
  "v_exp_f32 v180, v180\n" \
  "v_exp_f32 v181, v181\n" \
  "v_exp_f32 v182, v182\n" \
  "v_exp_f32 v183, v183\n" \
  "v_exp_f32 v184, v184\n" \
  "v_exp_f32 v185, v185\n" \
  "v_exp_f32 v186, v186\n" \
  "v_exp_f32 v187, v187\n" \
  "v_exp_f32 v188, v188\n" \
  "v_exp_f32 v189, v189\n" \
  "v_exp_f32 v190, v190\n" \
  "v_exp_f32 v191, v191\n" \
  "v_smfmac_f32_32x32x32_bf16 v[112:127], v[200:203], v[176:183], v30\n" \
  "s_nop 0\n" \
  "v_smfmac_f32_32x32x32_bf16 v[112:127], v[204:207], v[184:191], v30\n" \
  "s_nop 7\n" \
  "s_sub_u32 s55, s55, 1\n" \
  "s_cmp_lg_u32 s55, 0\n" \
  "s_cbranch_scc1 .Lfz_sl_b\n" \
  "s_mov_b32 s53, 1\n" \
  "s_mov_b32 s60, 12\n" \
  "s_nop 7\n" \
  "ds_write_b128 v12, v[64:67] offset:0\n" \
  "ds_write_b128 v12, v[68:71] offset:1024\n" \
  "ds_write_b128 v12, v[72:75] offset:2048\n" \
  "ds_write_b128 v12, v[76:79] offset:3072\n" \
  "ds_write_b128 v13, v[80:83] offset:0\n" \
  "ds_write_b128 v13, v[84:87] offset:1024\n" \
  "ds_write_b128 v13, v[88:91] offset:2048\n" \
  "ds_write_b128 v13, v[92:95] offset:3072\n" \
  "ds_write_b128 v14, v[96:99] offset:0\n" \
  "ds_write_b128 v14, v[100:103] offset:1024\n" \
  "ds_write_b128 v14, v[104:107] offset:2048\n" \
  "ds_write_b128 v14, v[108:111] offset:3072\n" \
  "s_branch .Lfz_red\n" \
    \
  ".Lfz_h1r:\n" \
  "s_cmp_eq_u32 s54, 0\n" \
  "s_cbranch_scc1 .Lfz_h1g\n" \
  "s_sub_u32 s54, s54, 1\n" \
  "s_sleep 8\n" \
  "buffer_load_dword v23, v11, s[36:39], 0 offen sc1\n" \
  "buffer_load_dwordx2 v[24:25], v242, s[36:39], s35 offen sc1\n" \
  "s_waitcnt vmcnt(0)\n" \
  "s_branch .Lfz_h1\n" \
  ".Lfz_csr:\n" \
  "s_cmp_eq_u32 s54, 0\n" \
  "s_cbranch_scc1 .Lfz_csg\n" \
  "s_sub_u32 s54, s54, 1\n" \
  "s_sleep 8\n" \
  "buffer_load_dwordx2 v[128:129], v243, s[36:39], s92 offen offset:0 sc1\n" \
  "buffer_load_dwordx2 v[130:131], v242, s[36:39], s92 offen offset:512 sc1\n" \
  "buffer_load_dwordx2 v[132:133], v243, s[36:39], s92 offen offset:1024 sc1\n" \
  "buffer_load_dwordx2 v[134:135], v242, s[36:39], s92 offen offset:1536 sc1\n" \
  "buffer_load_dwordx2 v[136:137], v243, s[36:39], s92 offen offset:2048 sc1\n" \
  "buffer_load_dwordx2 v[138:139], v242, s[36:39], s92 offen offset:2560 sc1\n" \
  "buffer_load_dwordx2 v[140:141], v243, s[36:39], s92 offen offset:3072 sc1\n" \
  "buffer_load_dwordx2 v[142:143], v242, s[36:39], s92 offen offset:3584 sc1\n" \
  "s_waitcnt vmcnt(0)\n" \
  "s_branch .Lfz_cs\n" \
  ".Lfz_hps:\n" \
  "s_mov_b32 s63, 0x100000\n" \
  ".Lfz_poll4:\n" \
  "buffer_load_dword v29, v26, s[36:39], 0 offen sc1\n" \
  "s_waitcnt vmcnt(0)\n" \
  "v_cmp_ne_u32 vcc, s51, v29\n" \
  "s_sub_u32 s63, s63, 1\n" \
  "s_cbranch_vccz .Lfz_poll4x\n" \
  "s_sleep 4\n" \
  "s_cmp_lg_u32 s63, 0\n" \
  "s_cbranch_scc1 .Lfz_poll4\n" \
  ".Lfz_poll4x:\n" \
  "s_branch .Lfz_hpx\n" \
  ".Lfz_oret0:\n" \
  "s_cmp_eq_u32 s63, 0\n" \
  "s_cbranch_scc1 .Lfz_ogo0\n" \
  "s_sub_u32 s63, s63, 1\n" \
  "s_sleep 1\n" \
  "buffer_load_dwordx4 v[48:51], v24, s[36:39], s54 offen offset:0 sc1\n" \
  "buffer_load_dwordx4 v[52:55], v24, s[36:39], s54 offen offset:1024 sc1\n" \
  "buffer_load_dwordx4 v[56:59], v24, s[36:39], s54 offen offset:2048 sc1\n" \
  "buffer_load_dwordx4 v[60:63], v24, s[36:39], s54 offen offset:3072 sc1\n" \
  "s_waitcnt vmcnt(0)\n" \
  "v_max3_u32 v29, v48, v49, v50\n" \
  "v_max3_u32 v30, v51, v52, v53\n" \
  "v_max3_u32 v29, v54, v55, v29\n" \
  "v_max3_u32 v30, v56, v57, v30\n" \
  "v_max3_u32 v29, v58, v59, v29\n" \
  "v_max3_u32 v30, v60, v61, v30\n" \
  "v_max3_u32 v29, v62, v63, v29\n" \
  "v_max_u32 v29, v29, v30\n" \
  "v_cmp_eq_u32 vcc, -1, v29\n" \
  "s_cbranch_vccnz .Lfz_oret0\n" \
  "s_branch .Lfz_ogo0\n" \
  ".Lfz_oret1:\n" \
  "s_cmp_eq_u32 s63, 0\n" \
  "s_cbranch_scc1 .Lfz_ogo1\n" \
  "s_sub_u32 s63, s63, 1\n" \
  "s_sleep 1\n" \
  "buffer_load_dwordx4 v[64:67], v24, s[36:39], s55 offen offset:0 sc1\n" \
  "buffer_load_dwordx4 v[68:71], v24, s[36:39], s55 offen offset:1024 sc1\n" \
  "buffer_load_dwordx4 v[72:75], v24, s[36:39], s55 offen offset:2048 sc1\n" \
  "buffer_load_dwordx4 v[76:79], v24, s[36:39], s55 offen offset:3072 sc1\n" \
  "s_waitcnt vmcnt(0)\n" \
  "v_max3_u32 v29, v64, v65, v66\n" \
  "v_max3_u32 v30, v67, v68, v69\n" \
  "v_max3_u32 v29, v70, v71, v29\n" \
  "v_max3_u32 v30, v72, v73, v30\n" \
  "v_max3_u32 v29, v74, v75, v29\n" \
  "v_max3_u32 v30, v76, v77, v30\n" \
  "v_max3_u32 v29, v78, v79, v29\n" \
  "v_max_u32 v29, v29, v30\n" \
  "v_cmp_eq_u32 vcc, -1, v29\n" \
  "s_cbranch_vccnz .Lfz_oret1\n" \
  "s_branch .Lfz_ogo1\n" \
  ".Lfz_oret2:\n" \
  "s_cmp_eq_u32 s63, 0\n" \
  "s_cbranch_scc1 .Lfz_ogo2\n" \
  "s_sub_u32 s63, s63, 1\n" \
  "s_sleep 1\n" \
  "buffer_load_dwordx4 v[80:83], v24, s[36:39], s56 offen offset:0 sc1\n" \
  "buffer_load_dwordx4 v[84:87], v24, s[36:39], s56 offen offset:1024 sc1\n" \
  "buffer_load_dwordx4 v[88:91], v24, s[36:39], s56 offen offset:2048 sc1\n" \
  "buffer_load_dwordx4 v[92:95], v24, s[36:39], s56 offen offset:3072 sc1\n" \
  "s_waitcnt vmcnt(0)\n" \
  "v_max3_u32 v29, v80, v81, v82\n" \
  "v_max3_u32 v30, v83, v84, v85\n" \
  "v_max3_u32 v29, v86, v87, v29\n" \
  "v_max3_u32 v30, v88, v89, v30\n" \
  "v_max3_u32 v29, v90, v91, v29\n" \
  "v_max3_u32 v30, v92, v93, v30\n" \
  "v_max3_u32 v29, v94, v95, v29\n" \
  "v_max_u32 v29, v29, v30\n" \
  "v_cmp_eq_u32 vcc, -1, v29\n" \
  "s_cbranch_vccnz .Lfz_oret2\n" \
  "s_branch .Lfz_ogo2\n" \
  ".Lfz_oret3:\n" \
  "s_cmp_eq_u32 s63, 0\n" \
  "s_cbranch_scc1 .Lfz_ogo3\n" \
  "s_sub_u32 s63, s63, 1\n" \
  "s_sleep 1\n" \
  "buffer_load_dwordx4 v[96:99], v24, s[36:39], s57 offen offset:0 sc1\n" \
  "buffer_load_dwordx4 v[100:103], v24, s[36:39], s57 offen offset:1024 sc1\n" \
  "buffer_load_dwordx4 v[104:107], v24, s[36:39], s57 offen offset:2048 sc1\n" \
  "buffer_load_dwordx4 v[108:111], v24, s[36:39], s57 offen offset:3072 sc1\n" \
  "s_waitcnt vmcnt(0)\n" \
  "v_max3_u32 v29, v96, v97, v98\n" \
  "v_max3_u32 v30, v99, v100, v101\n" \
  "v_max3_u32 v29, v102, v103, v29\n" \
  "v_max3_u32 v30, v104, v105, v30\n" \
  "v_max3_u32 v29, v106, v107, v29\n" \
  "v_max3_u32 v30, v108, v109, v30\n" \
  "v_max3_u32 v29, v110, v111, v29\n" \
  "v_max_u32 v29, v29, v30\n" \
  "v_cmp_eq_u32 vcc, -1, v29\n" \
  "s_cbranch_vccnz .Lfz_oret3\n" \
  "s_branch .Lfz_ogo3\n" \
  ".Lfz_end:\n"
#define FUSED_CLOB "v1", "v2", "v3", "v4", "v5", "v6", "v7", "v8", "v9", "v10", "v11", "v12", "v13", "v14", "v15", "v16", "v17", "v18", "v19", "v20", "v21", "v22", "v23", "v24", "v25", "v26", "v27", "v28", "v29", "v30", "v31", "v32", "v33", "v34", "v35", "v36", "v37", "v38", "v39", "v40", "v41", "v42", "v43", "v44", "v45", "v46", "v47", "v48", "v49", "v50", "v51", "v52", "v53", "v54", "v55", "v56", "v57", "v58", "v59", "v60", "v61", "v62", "v63", "v64", "v65", "v66", "v67", "v68", "v69", "v70", "v71", "v72", "v73", "v74", "v75", "v76", "v77", "v78", "v79", "v80", "v81", "v82", "v83", "v84", "v85", "v86", "v87", "v88", "v89", "v90", "v91", "v92", "v93", "v94", "v95", "v96", "v97", "v98", "v99", "v100", "v101", "v102", "v103", "v104", "v105", "v106", "v107", "v108", "v109", "v110", "v111", "v112", "v113", "v114", "v115", "v116", "v117", "v118", "v119", "v120", "v121", "v122", "v123", "v124", "v125", "v126", "v127", "v128", "v129", "v130", "v131", "v132", "v133", "v134", "v135", "v136", "v137", "v138", "v139", "v140", "v141", "v142", "v143", "v144", "v145", "v146", "v147", "v148", "v149", "v150", "v151", "v152", "v153", "v154", "v155", "v156", "v157", "v158", "v159", "v160", "v161", "v162", "v163", "v164", "v165", "v166", "v167", "v168", "v169", "v170", "v171", "v172", "v173", "v174", "v175", "v176", "v177", "v178", "v179", "v180", "v181", "v182", "v183", "v184", "v185", "v186", "v187", "v188", "v189", "v190", "v191", "v192", "v193", "v194", "v195", "v196", "v197", "v198", "v199", "v200", "v201", "v202", "v203", "v204", "v205", "v206", "v207", "v208", "v209", "v210", "v211", "v212", "v213", "v214", "v215", "v216", "v217", "v218", "v219", "v220", "v221", "v222", "v223", "v224", "v225", "v226", "v227", "v228", "v229", "v230", "v231", "v232", "v233", "v234", "v235", "v236", "v237", "v238", "v239", "v240", "v241", "v242", "v243", "v244", "v245", "v246", "v247", "v248", "v249", "v250", "v251", "v252", "v253", "v254", "v255", "s4", "s5", "s6", "s7", "s8", "s9", "s10", "s11", "s12", "s13", "s14", "s15", "s16", "s17", "s18", "s19", "s20", "s21", "s22", "s23", "s24", "s25", "s26", "s27", "s28", "s29", "s30", "s31", "s33", "s34", "s35", "s36", "s37", "s38", "s39", "s40", "s41", "s42", "s43", "s44", "s45", "s46", "s47", "s48", "s49", "s50", "s51", "s52", "s53", "s54", "s55", "s56", "s57", "s58", "s59", "s60", "s61", "s62", "s63", "s64", "s65", "s66", "s67", "s68", "s69", "s70", "s71", "s72", "s73", "s74", "s75", "s76", "s77", "s78", "s79", "s80", "s81", "s82", "s83", "s84", "s85", "s86", "s87", "s88", "s89", "s90", "s91", "s92", "s93", "s94", "s95", "s96", "s97", "s98", "s99", "vcc", "scc", "memory"
constexpr unsigned WS_BYTES = 9982080;
constexpr int LDS_BYTES = 163840;

#define BUF(n) const void* n, unsigned n##_bytes, unsigned n##_fmt
__global__ __launch_bounds__(512, 2) void fused(BUF(x), BUF(wq), BUF(wk), BUF(wv), BUF(wo), BUF(wg), BUF(wu), BUF(wd), BUF(ws), BUF(out)) {
  asm volatile(FUSED_ASM :: "{s[0:1]}"(__builtin_amdgcn_kernarg_segment_ptr()), "{s2}"(blockIdx.x), "{v0}"(threadIdx.x) : FUSED_CLOB);
}

extern "C" void kernel_launch(void* const* d_in, const int* in_sizes, int n_in, void* d_out, int out_size, void* d_ws, size_t ws_size, hipStream_t stream) {
  (void)hipFuncSetAttribute((const void*)fused, hipFuncAttributeMaxDynamicSharedMemorySize, LDS_BYTES);
  const unsigned F = 0x00020000u;
#define IN(i) d_in[i], 2u * (unsigned)in_sizes[i], F
  fused<<<256, 512, LDS_BYTES, stream>>>(IN(0), IN(1), IN(2), IN(3), IN(4), IN(6), IN(7), IN(8), d_ws, WS_BYTES, F, d_out, 2u * (unsigned)out_size, F);
}
```
